```python
import torch

def _softmax(x, axis=-1):
    return torch.softmax(x, dim=axis)
def _gelu(x):
    return torch.nn.functional.gelu(x, approximate="tanh")
def _silu(x):
    return torch.nn.functional.silu(x)
def _sigmoid(x):
    return torch.sigmoid(x)
def _layernorm(x, axis=-1, eps=1e-5):
    m = x.mean(dim=axis, keepdim=True)
    v = x.var(dim=axis, keepdim=True, unbiased=False)
    return (x - m) / torch.sqrt(v + eps)
def _scatter_add(tgt, idx, src, axis=0):
    return tgt.clone().index_add_(axis, idx, src)
def _segment_sum(src, seg, n):
    out = torch.zeros((n,) + src.shape[1:], dtype=src.dtype)
    return out.index_add_(0, seg, src)
def _pad(x, pw):
    flat = tuple(v for pair in reversed(pw) for v in pair)
    return torch.nn.functional.pad(x, flat)

kernel_name = 'parallel_sum__ssm_conv_s4d__glu_gated_bilinear__prompt'

def setup_inputs():

    _w = (lambda a: (lambda t: t.to(torch.bfloat16) if t.is_floating_point() else t)(torch.as_tensor(a)))
    def _A_setup():
        Bsz, L, D, N = 1, 2048, 512, 128
        return {
            "u": _w((lambda *s: torch.randn(*s, dtype=torch.bfloat16) * 0.05)(Bsz, L, D)),
            "log_A_real": _w((lambda *s: torch.randn(*s, dtype=torch.bfloat16) * 6.4)(D, N) * 0.5),
            "A_imag": _w((lambda *s: torch.randn(*s, dtype=torch.bfloat16) * 0.05)(D, N) * 3.14),
            "C_real": _w((lambda *s: torch.randn(*s, dtype=torch.bfloat16) * 0.05)(D, N) / N ** 0.5),
            "C_imag": _w((lambda *s: torch.randn(*s, dtype=torch.bfloat16) * 0.05)(D, N) / N ** 0.5),
            "log_dt": _w((lambda *s: torch.randn(*s, dtype=torch.bfloat16) * 0.05)(D) * 0.5 - 4.0),
            "D_skip": _w((lambda *s: torch.randn(*s, dtype=torch.bfloat16) * 0.05)(D)),
        }
    _A_in = _A_setup()

    def _B_setup():
        B, S, D, F = 1, 2048, 512, 1792
        return {
            "x": _w((lambda *s: torch.randn(*s, dtype=torch.bfloat16) * 0.05)(B, S, D)),
            "w_gate": _w((lambda *s: torch.randn(*s, dtype=torch.bfloat16) * 0.4)(D, F) * (D ** -0.5)),
            "w_up": _w((lambda *s: torch.randn(*s, dtype=torch.bfloat16) * 0.4)(D, F) * (D ** -0.5)),
            "w_down": _w((lambda *s: torch.randn(*s, dtype=torch.bfloat16) * 0.4)(F, D) * (F ** -0.5)),
        }
    _B_in = _B_setup()

    return {
        "A_u": _A_in["u"],
        "A_log_A_real": _A_in["log_A_real"],
        "A_A_imag": _A_in["A_imag"],
        "A_C_real": _A_in["C_real"],
        "A_C_imag": _A_in["C_imag"],
        "A_log_dt": _A_in["log_dt"],
        "A_D_skip": _A_in["D_skip"],
        "B_x": _B_in["x"],
        "B_w_gate": _B_in["w_gate"],
        "B_w_up": _B_in["w_up"],
        "B_w_down": _B_in["w_down"],
    }

def reference(A_u, A_log_A_real, A_A_imag, A_C_real, A_C_imag, A_log_dt, A_D_skip, B_x, B_w_gate, B_w_up, B_w_down):
    def _A_ref(u, log_A_real, A_imag, C_real, C_imag, log_dt, D_skip):
        L = u.shape[1]
        dt = torch.exp(log_dt)
        A = -torch.exp(log_A_real) + 1j * A_imag
        C = (C_real + 1j * C_imag) * dt[:, None]
        pos = (lambda x, dt: x.to(dt))(torch.arange(L), log_dt.dtype)
        P = torch.exp((A * dt[:, None])[:, :, None] * pos[None, None, :])
        K = 2.0 * torch.einsum("dn,dnl->dl", C, P).real
        L2 = 2 * L
        u_t = (lambda x, *d: x.permute(*d))(u, 0, 2, 1)
        Kf = (lambda x, *a, **k: torch.fft.rfft(x.float(), *a, **k))(K, L2)
        Uf = (lambda x, *a, **k: torch.fft.rfft(x.float(), *a, **k))(u_t, L2)
        y = (lambda x, *a, **k: torch.fft.irfft(x, *a, **k).to(torch.bfloat16))(Uf * Kf[None, :, :], L2)[:, :, :L]
        y = (lambda x, *d: x.permute(*d))(y, 0, 2, 1)
        return y + u * D_skip[None, None, :]

    def _B_ref(x, w_gate, w_up, w_down):
        g = torch.einsum("bsd,df->bsf", x, w_gate)
        u = torch.einsum("bsd,df->bsf", x, w_up)
        h = g * u
        return torch.einsum("bsf,fd->bsd", h, w_down)

    x = A_u
    xn = _layernorm(x, axis=-1)
    return x + _A_ref(xn, A_log_A_real, A_A_imag, A_C_real, A_C_imag, A_log_dt, A_D_skip) + _B_ref(xn, B_w_gate, B_w_up, B_w_down)
```

```cpp
#include <hip/hip_runtime.h>
#include <hip/hip_bf16.h>
#define POSRND 1
typedef unsigned short u16;
__device__ __forceinline__ float b2f(u16 v){ return __uint_as_float(((unsigned)v)<<16); }
__device__ __forceinline__ u16 f2b(float f){ unsigned u=__float_as_uint(f); return (u16)((u + 0x7FFF + ((u>>16)&1))>>16); }
__device__ __forceinline__ float rb(float f){ return b2f(f2b(f)); }
constexpr int L=2048,D=512,N=128,F=1792;

__global__ void ln_k(const u16* x, u16* xn){
  int t=blockIdx.x; int tid=threadIdx.x;
  float v[8]; float s=0;
  for(int i=0;i<8;i++){ v[i]=b2f(x[t*D+tid*8+i]); s+=v[i]; }
  for(int o=32;o>0;o>>=1) s+=__shfl_xor(s,o);
  float mean=s/D; float q=0;
  for(int i=0;i<8;i++){ float d=v[i]-mean; q+=d*d; }
  for(int o=32;o>0;o>>=1) q+=__shfl_xor(q,o);
  float m=rb(mean), var=rb(q/D);
  float sd=rb(sqrtf(rb(var+1e-5f)));
  for(int i=0;i<8;i++){ xn[t*D+tid*8+i]=f2b(rb(v[i]-m)/sd); }
}
__global__ void kgen_k(const u16* lar,const u16* aim,const u16* cr,const u16* ci,const u16* ldt,float* K){
  int d=blockIdx.y; int l=blockIdx.x*256+threadIdx.x;
  float dt=rb(expf(b2f(ldt[d])));
  float pos = POSRND ? rb((float)l) : (float)l;
  float acc=0;
  for(int n=0;n<N;n++){
    float ar=-rb(expf(b2f(lar[d*N+n])))*dt; float ai=b2f(aim[d*N+n])*dt;
    float c_r=b2f(cr[d*N+n])*dt, c_i=b2f(ci[d*N+n])*dt;
    float e=expf(ar*pos); float sn,cs; sincosf(ai*pos,&sn,&cs);
    acc+= c_r*e*cs - c_i*e*sn;
  }
  K[d*L+l]=2.f*acc;
}
__global__ void conv_k(const float* K,const u16* xn,float* y){
  int d=blockIdx.x*64+(threadIdx.x&63); int t=blockIdx.y*4+(threadIdx.x>>6);
  float acc=0;
  for(int s=0;s<=t;s++) acc+=K[d*L+t-s]*b2f(xn[s*D+d]);
  y[t*D+d]=acc;
}
__global__ void g1_k(const u16* xn,const u16* wg,const u16* wu,u16* h){
  int f=blockIdx.x*64+(threadIdx.x&63); int t=blockIdx.y*4+(threadIdx.x>>6);
  float g=0,u=0;
  for(int k=0;k<D;k++){ float a=b2f(xn[t*D+k]); g+=a*b2f(wg[k*F+f]); u+=a*b2f(wu[k*F+f]); }
  h[t*F+f]=f2b(rb(g)*rb(u));
}
__global__ void g2_k(const u16* h,const u16* wd,const u16* x,const u16* xn,const float* y,const u16* dsk,u16* out){
  int d=blockIdx.x*64+(threadIdx.x&63); int t=blockIdx.y*4+(threadIdx.x>>6);
  float a=0;
  for(int k=0;k<F;k++) a+=b2f(h[t*F+k])*b2f(wd[k*D+d]);
  float A=rb(rb(y[t*D+d])+rb(b2f(xn[t*D+d])*b2f(dsk[d])));
  out[t*D+d]=f2b(rb(b2f(x[t*D+d])+A)+rb(a));
}
extern "C" void kernel_launch(void* const* d_in, const int* in_sizes, int n_in, void* d_out, int out_size, void* d_ws, size_t ws_size, hipStream_t stream){
  const u16* x=(const u16*)d_in[0];
  char* ws=(char*)d_ws;
  u16* xn=(u16*)ws; float* K=(float*)(ws+(4<<20)); float* y=(float*)(ws+(16<<20)); u16* h=(u16*)(ws+(32<<20));
  ln_k<<<L,64,0,stream>>>(x,xn);
  kgen_k<<<dim3(L/256,D),256,0,stream>>>((const u16*)d_in[1],(const u16*)d_in[2],(const u16*)d_in[3],(const u16*)d_in[4],(const u16*)d_in[5],K);
  conv_k<<<dim3(D/64,L/4),256,0,stream>>>(K,xn,y);
  g1_k<<<dim3(F/64,L/4),256,0,stream>>>(xn,(const u16*)d_in[8],(const u16*)d_in[9],h);
  g2_k<<<dim3(D/64,L/4),256,0,stream>>>(h,(const u16*)d_in[10],x,xn,y,(const u16*)d_in[6],(u16*)d_out);
}
```

```cpp
#include <hip/hip_runtime.h>
#include <hip/hip_bf16.h>

typedef unsigned short u16;
typedef unsigned int u32;
typedef __attribute__((ext_vector_type(8))) __bf16 bf8;
typedef __attribute__((ext_vector_type(2))) __bf16 bf2;
typedef __attribute__((ext_vector_type(8))) _Float16 h8;
typedef __attribute__((ext_vector_type(2))) _Float16 h2;
typedef __attribute__((ext_vector_type(4))) u32 u4;
typedef __attribute__((ext_vector_type(2))) u32 u2;
typedef __attribute__((ext_vector_type(4))) short s4;
typedef __attribute__((ext_vector_type(16))) float f16v;
typedef __attribute__((ext_vector_type(4))) float f4;
typedef __attribute__((ext_vector_type(2))) float f2;
#define LAS __attribute__((address_space(3)))
#define DEV __device__ __forceinline__

constexpr int L = 2048, D = 512, NM = 128, F = 1792;
constexpr size_t WS_H = 0;
constexpr size_t WS_WD = WS_H + (size_t)64 * 112 * 1024;
constexpr size_t WS_XT = WS_WD + (size_t)16 * 112 * 1024;
constexpr size_t WS_YT = WS_XT + (size_t)2 * 1024 * 1024;

DEV float b2f(u16 v) { return __uint_as_float(((u32)v) << 16); }
DEV float blo(u32 v) { return __uint_as_float(v << 16); }
DEV float bhi(u32 v) { return __uint_as_float(v & 0xffff0000u); }
DEV u32 pk_bf16(float a, float b) { f2 v = {a, b}; return __builtin_bit_cast(u32, __builtin_convertvector(v, bf2)); }
DEV u32 pk_f16(float a, float b) { f2 v = {a, b}; return __builtin_bit_cast(u32, __builtin_convertvector(v, h2)); }
DEV bf2 as_bf2(u32 v) { return __builtin_bit_cast(bf2, v); }
DEV h2 as_h2(u32 v) { return __builtin_bit_cast(h2, v); }
DEV float rbf(float f) { return blo(pk_bf16(f, f)); }

DEV void dma16(u32 lds, const void* sbase, u32 voff) {
  asm volatile("s_mov_b32 m0, %0\n\ts_nop 0\n\tglobal_load_lds_dwordx4 %1, %2" ::"s"(lds), "v"(voff), "s"(sbase) : "memory");
}
#define WAIT_VM(n) asm volatile("s_waitcnt vmcnt(" #n ")" ::: "memory")
#define WAIT_LGKM0() asm volatile("s_waitcnt lgkmcnt(0)" ::: "memory")
#define BAR() asm volatile("s_barrier" ::: "memory")

#define ST_DECL
#define ST(i)
#define ST_OUT(kid)
DEV u4 lds_r128(u32 a) { return *(LAS u4*)a; }
DEV u2 lds_r64(u32 a) { return *(LAS u2*)a; }
DEV u32 lds_r32(u32 a) { return *(LAS u32*)a; }
DEV void lds_w128(u32 a, u4 v) { *(LAS u4*)a = v; }
DEV void lds_w64(u32 a, u2 v) { *(LAS u2*)a = v; }
DEV void lds_w32(u32 a, u32 v) { *(LAS u32*)a = v; }
DEV u2 lds_tr(u32 a) { return __builtin_bit_cast(u2, __builtin_amdgcn_ds_read_tr16_b64_v4i16((LAS s4*)a)); }

constexpr int RW = 40960;

DEV u4 norm_bf16(u4 v, float r, float c) {
  u4 o;
#pragma unroll
  for (int e = 0; e < 4; e++) o[e] = pk_bf16(__builtin_fmaf(blo(v[e]), r, c), __builtin_fmaf(bhi(v[e]), r, c));
  return o;
}
DEV u4 norm_f16(u4 v, float r, float c) {
  u4 o;
#pragma unroll
  for (int e = 0; e < 4; e++) o[e] = pk_f16(__builtin_fmaf(blo(v[e]), r, c), __builtin_fmaf(bhi(v[e]), r, c));
  return o;
}

__global__ __launch_bounds__(256, 1) void k1(const u16* __restrict__ x, const u16* __restrict__ wg,
                                             const u16* __restrict__ wu, const u16* __restrict__ wd,
                                             char* __restrict__ ws) {
  extern __shared__ __attribute__((aligned(16))) char sm[];
  const u32 sm0 = (u32)(size_t)(LAS char*)sm;
  const int b = blockIdx.x, xcd = b & 7, q = b >> 3;
  const int ti = (xcd >> 1) * 4 + (q >> 3), tj = (xcd & 1) * 8 + (q & 7);
  const int w = __builtin_amdgcn_readfirstlane(threadIdx.x >> 6);
  const int lane = threadIdx.x & 63;
  const int sig = (4 * (ti & 3) + (ti >> 2) + tj) & 15;
  const int rot = sig >> 1;
  const u32 rw = sm0 + w * RW;
  ST_DECL ST(0);

  {
    const u32 ve = (lane >> 3) * 1024 + (((lane & 7) ^ (lane >> 4)) << 4);
    const u32 vo = (8 + (lane >> 3)) * 1024 + (((lane & 7) ^ (4 + (lane >> 4))) << 4);
    const char* xb = (const char*)x + (size_t)(ti * 128 + w * 32) * 1024;
#pragma unroll
    for (int c = 0; c < 8; c++) {
      const char* p = xb + ((c + rot) & 7) * 128;
      dma16(rw + c * 4096, p, ve);
      dma16(rw + c * 4096 + 1024, p, vo);
      dma16(rw + c * 4096 + 2048, p + 16384, ve);
      dma16(rw + c * 4096 + 3072, p + 16384, vo);
    }
  }
  u32 wv[7];
#pragma unroll
  for (int ii = 0; ii < 7; ii++) {
    u32 P = 64 * ii + lane;
    u32 rr = (P * 4682u) >> 16;
    wv[ii] = rr * 3584 + (P - rr * 14) * 16;
  }
  const char* wb = (const char*)((w & 2) ? wu : wg) + (size_t)(32 * (w & 1)) * 3584 + tj * 224;
  const u32 wdst = rw + ((w & 2) ? 128 : 0);
  auto stageW = [&](int s) {
    const char* p = wb + (size_t)((s + rot) & 7) * (64 * 3584);
    const u32 d = wdst + ((s + 4) % 5) * 8192;
#pragma unroll
    for (int ii = 0; ii < 7; ii++) dma16(d + ii * 1024, p, wv[ii]);
  };
  stageW(0);
  ST(1);

  u4 xr[32];
  float s1 = 0.f, s2 = 0.f, s1b = 0.f, s2b = 0.f;
  {
    const int r = lane & 31, h = lane >> 5, z = (r >> 1) & 7;
    u32 xa[4];
#pragma unroll
    for (int s = 0; s < 4; s++) xa[s] = rw + r * 128 + (((2 * s + h) ^ z) << 4);
    const bf2 ones = __builtin_bit_cast(bf2, 0x3f803f80u);
#pragma unroll
    for (int cc = 0; cc < 4; cc++) {
      if (cc == 0) WAIT_VM(31);
      if (cc == 1) WAIT_VM(30);
      if (cc == 2) WAIT_VM(29);
      if (cc == 3) WAIT_VM(28);
      if (cc == 0) ST(2);
      if (cc == 3) ST(3);
#pragma unroll
      for (int t = 0; t < 8; t++) xr[cc * 8 + t] = lds_r128(xa[t & 3] + (cc * 2 + (t >> 2)) * 4096);
      WAIT_LGKM0();
      if (cc < 3) stageW(cc + 1);
#pragma unroll
      for (int t = 0; t < 8; t++) {
#pragma unroll
        for (int e = 0; e < 4; e += 2) {
          bf2 v = as_bf2(xr[cc * 8 + t][e]);
          bf2 u = as_bf2(xr[cc * 8 + t][e + 1]);
          s1 = __builtin_amdgcn_fdot2_f32_bf16(v, ones, s1, false);
          s2 = __builtin_amdgcn_fdot2_f32_bf16(v, v, s2, false);
          s1b = __builtin_amdgcn_fdot2_f32_bf16(u, ones, s1b, false);
          s2b = __builtin_amdgcn_fdot2_f32_bf16(u, u, s2b, false);
        }
      }
    }
  }
  s1 += s1b; s2 += s2b;
  s1 += __shfl_xor(s1, 32);
  s2 += __shfl_xor(s2, 32);
  float rs, rc;
  {
    float mean = s1 * (1.f / D);
    float var = fmaxf(s2 * (1.f / D) - mean * mean, 0.f);
    float sd = rbf(__builtin_sqrtf(rbf(rbf(var) + 1e-5f)));
    rs = 1.f / sd;
    rc = -rbf(mean) * rs;
  }
  {
    u4 fa = (sig & 1) ? xr[2] : xr[0], fb = (sig & 1) ? xr[3] : xr[1];
    fa = norm_f16(fa, rs, rc);
    fb = norm_f16(fb, rs, rc);
    const u32 sc = rw + 3 * 8192;
    const u32 wa = sc + (lane & 31) * 64 + (lane >> 5) * 16;
    lds_w128(wa, fa);
    lds_w128(wa + 32, fb);
    const int G = lane >> 4, qq = (lane & 15) >> 2, p = lane & 3, i = lane & 15;
    const u32 ra = sc + (8 * G + qq) * 64 + 8 * p;
    char* xt = ws + WS_XT + ((size_t)(sig * 16 + (i >> 1)) * 64 + ti * 4 + w) * 128 + (i & 1) * 64 + 16 * G;
    u2 a0 = lds_tr(ra), a1 = lds_tr(ra + 256), b0 = lds_tr(ra + 32), b1 = lds_tr(ra + 256 + 32);
    *(u4*)xt = u4{a0[0], a0[1], a1[0], a1[1]};
    *(u4*)(xt + 8 * 64 * 128) = u4{b0[0], b0[1], b1[0], b1[1]};
    WAIT_LGKM0();
  }
  stageW(4);
  ST(4);

  f16v acc[7];
#pragma unroll
  for (int c = 0; c < 7; c++) acc[c] = f16v{};
  u32 ta0, ta1;
  {
    const int G = lane >> 4, h = G >> 1, gb = G & 1, qq = (lane & 15) >> 2, p = lane & 3;
    ta0 = sm0 + (p >> 1) * (2 * RW + 128) + (8 * h + qq) * 224 + 16 * (p & 1) + 8 * gb;
    ta1 = ta0 + RW;
  }
  auto rdW = [&](int t, int c) -> u4 {
    const int s = t >> 2, kk = t & 3;
    const u32 off = ((s + 4) % 5) * 8192 + ((kk & 1) * 16) * 224 + 32 * c;
    const u32 base = (kk >> 1) ? ta1 : ta0;
    u2 lo = lds_tr(base + off), hi = lds_tr(base + off + 4 * 224);
    return u4{lo[0], lo[1], hi[0], hi[1]};
  };
  WAIT_VM(28);
  BAR();
  ST(5);
  u4 wc[7], wn[7];
#pragma unroll
  for (int c = 0; c < 7; c++) wc[c] = rdW(0, c);
  u4 xc = norm_bf16(xr[0], rs, rc), xn_;
  const int fid0 = b * 7 + w * 2;
#pragma unroll
  for (int t = 0; t < 32; t++) {
    const int s = t >> 2, kk = t & 3;
    if (t == 8) ST(6);
    if (t == 16) ST(7);
    if (t == 24) ST(8);
    if (kk == 2 && s < 7) {
      if (s == 0) WAIT_VM(21);
      else if (s <= 4) WAIT_VM(14);
      else if (s == 5) WAIT_VM(9);
      else WAIT_VM(2);
      BAR();
      if (s >= 1 && s <= 3) stageW(s + 4);
      if (s == 4) {
#pragma unroll
        for (int e = 0; e < 2; e++) {
          int fid = fid0 + e; if (w * 2 + e == 7) fid = fid0;
          const int cb = fid & 15, ks = fid >> 4;
          const char* p = (const char*)wd + (size_t)ks * 16 * 1024 + cb * 64;
          dma16(rw + 2 * 8192 + e * 1024, p, (u32)((lane >> 2) * 1024 + (lane & 3) * 16));
        }
      }
    }
    if (t < 31) {
#pragma unroll
      for (int c = 0; c < 7; c++) wn[c] = rdW(t + 1, c);
      xn_ = norm_bf16(xr[t + 1], rs, rc);
    }
#pragma unroll
    for (int c = 0; c < 7; c++)
      acc[c] = __builtin_amdgcn_mfma_f32_32x32x16_bf16(__builtin_bit_cast(bf8, wc[c]), __builtin_bit_cast(bf8, xc), acc[c], 0, 0, 0);
    if (t < 31) {
#pragma unroll
      for (int c = 0; c < 7; c++) {
        __builtin_amdgcn_sched_group_barrier(0x8, 1, 0);
        __builtin_amdgcn_sched_group_barrier(0x100, 2, 0);
        __builtin_amdgcn_sched_group_barrier(0x2, 3, 0);
      }
#pragma unroll
      for (int c = 0; c < 7; c++) wc[c] = wn[c];
      xc = xn_;
    }
    __builtin_amdgcn_sched_barrier(0);
  }
  ST(9);
  {
    char* hp = ws + WS_H + ((size_t)(ti * 4 + w) * 112 + tj * 7) * 1024 + lane * 16;
#pragma unroll
    for (int c = 0; c < 7; c++) {
      u4 o;
      o[0] = pk_bf16(acc[c][0] * acc[c][4], acc[c][1] * acc[c][5]);
      o[1] = pk_bf16(acc[c][2] * acc[c][6], acc[c][3] * acc[c][7]);
      o[2] = pk_bf16(acc[c][8] * acc[c][12], acc[c][9] * acc[c][13]);
      o[3] = pk_bf16(acc[c][10] * acc[c][14], acc[c][11] * acc[c][15]);
      *(u4*)(hp + c * 1024) = o;
    }
  }
  ST(10);
  {
    WAIT_VM(7);
    const int G = lane >> 4, qq = (lane & 15) >> 2, p = lane & 3;
    const u32 ra = rw + 2 * 8192 + (8 * (G >> 1) + qq) * 64 + 32 * (G & 1) + 8 * p;
#pragma unroll
    for (int e = 0; e < 2; e++) {
      if (w * 2 + e == 7) break;
      const int fid = fid0 + e, cb = fid & 15, ks = fid >> 4;
      u2 lo = lds_tr(ra + e * 1024), hi = lds_tr(ra + e * 1024 + 256);
      *(u4*)(ws + WS_WD + ((size_t)cb * 112 + ks) * 1024 + lane * 16) = u4{lo[0], lo[1], hi[0], hi[1]};
    }
  }
  ST(11); WAIT_VM(0); ST(12); ST_OUT(0);
}

struct cf { float r, i; };
DEV cf cmul(cf a, cf b) { return cf{a.r * b.r - a.i * b.i, a.r * b.i + a.i * b.r}; }
DEV cf cexp_at(float lr, float fr, float p) {
  float e = __builtin_amdgcn_exp2f(lr * p);
  float ph = __builtin_amdgcn_fractf(fr * p);
  return cf{e * __builtin_amdgcn_cosf(ph), e * __builtin_amdgcn_sinf(ph)};
}
constexpr int K2_XS = 0;
constexpr int K2_P = 2 * 4608;
constexpr int K2_C = K2_P + 2 * 8256;
constexpr int K2_UV = K2_C + 4 * 3072;
constexpr int K2_LDS = K2_UV + 4 * 14416;

DEV u32 cidx(int l) {
  u32 bits = pk_bf16((float)l, 0.f) & 0xffffu;
  return l < 128 ? (u32)l : bits - 17024u;
}

__global__ __launch_bounds__(256, 1) void k2(const u16* __restrict__ lar, const u16* __restrict__ aim,
                                             const u16* __restrict__ cr, const u16* __restrict__ ci,
                                             const u16* __restrict__ ldt, const u16* __restrict__ dsk,
                                             char* __restrict__ ws) {
  extern __shared__ __attribute__((aligned(16))) char sm[];
  const u32 sm0 = (u32)(size_t)(LAS char*)sm;
  const int pr = blockIdx.x;
  const int w = __builtin_amdgcn_readfirstlane(threadIdx.x >> 6);
  const int lane = threadIdx.x & 63;
  const int chl = w >> 1, hw = w & 1, ch = pr * 2 + chl;
  const u32 xs = sm0 + K2_XS + chl * 4608, Pt = sm0 + K2_P + chl * 8256 + 64;
  ST_DECL ST(0);
  const int n = ch * NM + hw * 64 + lane, n2 = ch * NM + (hw ^ 1) * 64 + lane;
  const float v_lar = b2f(lar[n]), v_aim = b2f(aim[n]), v_cr = b2f(cr[n]), v_ci = b2f(ci[n]);
  const float v_cr2 = b2f(cr[n2]), v_ci2 = b2f(ci[n2]);
  const float v_ldt = b2f(ldt[ch]), v_dsk = b2f(dsk[ch]);
  {
    const char* xb = ws + WS_XT + (size_t)pr * 8192 + chl * 64 + hw * 4096;
    const u32 vo = (lane >> 2) * 128 + (lane & 3) * 16;
    dma16(xs + 480 + hw * 2048, xb, vo);
    dma16(xs + 480 + hw * 2048 + 1024, xb + 2048, vo);
  }
  if (hw == 0) {
    if (lane < 30) lds_w128(xs + lane * 16, u4{0, 0, 0, 0});
    if (lane < 16) lds_w32(Pt - 64 + lane * 4, 0u);
  }
  const float dt = rbf(__expf(v_ldt));
  const float lr = -rbf(__expf(v_lar)) * dt * 1.44269504088896f;
  const float fr = v_aim * dt * 0.15915494309189535f;
  float cm = fmaxf(fmaxf(fabsf(v_cr), fabsf(v_ci)), fmaxf(fabsf(v_cr2), fabsf(v_ci2)));
#pragma unroll
  for (int o = 32; o > 0; o >>= 1) cm = fmaxf(cm, __shfl_xor(cm, o));
  const u32 eb = __float_as_uint(cm) >> 23;
  const float inv = __uint_as_float((254u - eb) << 23);
  const float kscale = 2.f * dt * __uint_as_float(eb << 23);
  const u32 uv = sm0 + K2_UV + w * 14416;
  ST(1);
  {
    const u32 wa = uv + lane * 4;
    auto putU = [&](int row, cf v) { lds_w32(wa + row * 272, pk_f16(v.r, -v.i)); };
    auto putV = [&](int col, cf v) { lds_w32(wa + (21 + col) * 272, pk_f16(v.r, v.i)); };
    cf e1 = cexp_at(lr, fr, 1.f), e8 = cexp_at(lr, fr, 8.f), e64 = cexp_at(lr, fr, 64.f), e256 = cexp_at(lr, fr, 256.f);
    cf e1024 = cexp_at(lr, fr, 1024.f);
    cf e2 = cmul(e1, e1), e4 = cmul(e2, e2), e16 = cmul(e8, e8), e32 = cmul(e16, e16), e128 = cmul(e64, e64);
    cf e512 = cmul(e256, e256);
    cf u[8];
    u[0] = cf{v_cr * inv, v_ci * inv};
    u[1] = cmul(u[0], e1); u[2] = cmul(u[0], e2); u[3] = cmul(u[1], e2);
#pragma unroll
    for (int r = 0; r < 4; r++) u[4 + r] = cmul(u[r], e4);
#pragma unroll
    for (int r = 0; r < 8; r++) putU(r, u[r]);
#pragma unroll
    for (int r = 0; r < 4; r++) putU(8 + r, cmul(u[2 * r], e256));
    cf a0 = cmul(u[0], e512), a1 = cmul(u[4], e512);
    putU(12, a0); putU(13, a1); putU(14, cmul(a0, e256)); putU(15, cmul(a1, e256));
    cf c0 = cmul(u[0], e1024), c2 = cmul(c0, e512);
    putU(16, c0); putU(17, cmul(c0, e256)); putU(18, c2); putU(19, cmul(c2, e256)); putU(20, cmul(c0, e1024));
    cf v[16];
    v[0] = cf{1.f, 0.f}; v[1] = e8; v[2] = e16; v[3] = cmul(e8, e16);
    v[4] = e32;
#pragma unroll
    for (int j = 1; j < 4; j++) v[4 + j] = cmul(v[j], e32);
    v[8] = e64;
#pragma unroll
    for (int j = 1; j < 8; j++) v[8 + j] = cmul(v[j], e64);
#pragma unroll
    for (int j = 0; j < 16; j++) putV(j, v[j]);
    putV(16, e128);
#pragma unroll
    for (int j = 1; j < 16; j++) putV(16 + j, cmul(v[j], e128));
  }
  ST(2);
  {
    f16v d = f16v{};
    const u32 ra = uv + (lane & 31) * 272 + (lane >> 5) * 16;
#pragma unroll
    for (int s = 0; s < 8; s++) {
      u4 a = lds_r128(ra + s * 32), bb = lds_r128(ra + 21 * 272 + s * 32);
      d = __builtin_amdgcn_mfma_f32_32x32x16_f16(__builtin_bit_cast(h8, a), __builtin_bit_cast(h8, bb), d, 0, 0, 0);
    }
    const u32 ct = sm0 + K2_C + w * 3072;
    const int j = lane & 31, h = lane >> 5;
    *(LAS f4*)(ct + (8 * j + 4 * h) * 4) = f4{d[0], d[1], d[2], d[3]};
    const u32 a1 = h ? (384 + 2 * j) : (256 + 4 * j), a2 = h ? (448 + 2 * j) : (256 + 4 * j + 2);
    *(LAS f2*)(ct + a1 * 4) = f2{d[4], d[5]};
    *(LAS f2*)(ct + a2 * 4) = f2{d[6], d[7]};
#pragma unroll
    for (int e = 0; e < 4; e++) *(LAS float*)(ct + (512 + 128 * h + 32 * e + j) * 4) = d[8 + e];
  }
  WAIT_LGKM0();
  BAR();
  ST(3);
  {
    const u32 c0 = sm0 + K2_C + chl * 2 * 3072;
#pragma unroll
    for (int i = 0; i < 16; i++) {
      const int l = hw * 1024 + i * 64 + lane;
      const u32 i0 = cidx(l), i1 = cidx(l > 0 ? l - 1 : 0);
      float k0 = *(LAS float*)(c0 + i0 * 4) + *(LAS float*)(c0 + 3072 + i0 * 4);
      float k1v = *(LAS float*)(c0 + i1 * 4) + *(LAS float*)(c0 + 3072 + i1 * 4);
      if (l == 0) k1v = 0.f;
      lds_w32(Pt + l * 4, pk_f16(k0, k1v));
    }
  }
  ST(4);
  WAIT_VM(0);
  WAIT_LGKM0();
  BAR();
  ST(5);
  f4 acc[8];
#pragma unroll
  for (int j = 0; j < 8; j++) acc[j] = f4{};
  const int a = lane & 15, g = lane >> 4;
  {
    const u32 ba = xs + 2 * (240 + 16 * a + 8 * g - 16);
    const u32 pa = Pt + 4 * (a - 8 * g + 16 - 6);
#pragma unroll 1
    for (int r = hw * 4; r < hw * 4 + 4; r++) {
      u4 bf[8];
#pragma unroll
      for (int i = 0; i < 8; i++) bf[i] = lds_r128(ba - 64 * r + 512 * i);
#pragma unroll
      for (int qq = 0; qq < 8; qq++) {
        const u32 p = pa + 128 * r + 1024 * qq;
        u4 tf = u4{lds_r32(p + 24), lds_r32(p + 16), lds_r32(p + 8), lds_r32(p)};
#pragma unroll
        for (int j = qq; j < 8; j++)
          acc[j] = __builtin_amdgcn_mfma_f32_16x16x32_f16(__builtin_bit_cast(h8, tf), __builtin_bit_cast(h8, bf[j - qq]), acc[j], 0, 0, 0);
      }
    }
  }
  ST(6);
  {
    const u32 mine = sm0 + K2_UV + w * 14416 + lane * 16, other = sm0 + K2_UV + (w ^ 1) * 14416 + lane * 16;
    f4 keep[4];
#pragma unroll
    for (int j = 0; j < 4; j++) {
      f4 snd = hw ? acc[j] : acc[4 + j];
      keep[j] = hw ? acc[4 + j] : acc[j];
      *(LAS f4*)(mine + j * 1024) = snd;
    }
    WAIT_LGKM0();
    BAR();
#pragma unroll
    for (int j = 0; j < 4; j++) {
      f4 v = keep[j] + *(LAS f4*)(other + j * 1024);
      const int jj = hw * 4 + j, t = 256 * jj + 16 * a + 4 * g;
      u2 xv = lds_r64(xs + 480 + 2 * t);
      h2 x01 = as_h2(xv[0]), x23 = as_h2(xv[1]);
      u2 o;
      o[0] = pk_bf16(v[0] * kscale + v_dsk * (float)x01[0], v[1] * kscale + v_dsk * (float)x01[1]);
      o[1] = pk_bf16(v[2] * kscale + v_dsk * (float)x23[0], v[3] * kscale + v_dsk * (float)x23[1]);
      *(u2*)(ws + WS_YT + ((size_t)(t >> 6) * 512 + ch) * 128 + (t & 63) * 2) = o;
    }
  }
  ST(7); WAIT_VM(0); ST(8); ST_OUT(1);
}

constexpr int K3_LDS = 8192 + 4 * 3 * 4096;
__global__ __launch_bounds__(256, 1) void k3(const u16* __restrict__ x, const char* __restrict__ ws, u16* __restrict__ out) {
  extern __shared__ __attribute__((aligned(16))) char sm[];
  const u32 sm0 = (u32)(size_t)(LAS char*)sm;
  const int b = blockIdx.x, xcd = b & 7, q = b >> 3;
  const int tb = xcd * 4 + (q >> 3), cb = q & 7;
  const int w = __builtin_amdgcn_readfirstlane(threadIdx.x >> 6);
  const int lane = threadIdx.x & 63;
  constexpr int KS = 28, DEPTH = 8;
  const u4* h0 = (const u4*)(ws + WS_H + ((size_t)(tb * 2) * 112 + w * KS) * 1024) + lane;
  const u4* h1 = h0 + 112 * 64;
  const u4* w0 = (const u4*)(ws + WS_WD + ((size_t)(cb * 2) * 112 + w * KS) * 1024) + lane;
  const u4* w1 = w0 + 112 * 64;
  ST_DECL ST(0);
  {
    const char* yb = ws + WS_YT + ((size_t)tb * 512 + cb * 64) * 128 + w * 2048;
    dma16(sm0 + w * 2048, yb, lane * 16);
    dma16(sm0 + w * 2048 + 1024, yb + 1024, lane * 16);
  }
  const int qt = w & 1, qc = w >> 1;
  const int tok = tb * 64 + qt * 32 + (lane & 31), h = lane >> 5;
  u2 xv[4];
#pragma unroll
  for (int gq = 0; gq < 4; gq++) xv[gq] = *(const u2*)(x + (size_t)tok * D + cb * 64 + qc * 32 + 8 * gq + 4 * h);
  f16v acc[4];
#pragma unroll
  for (int k = 0; k < 4; k++) acc[k] = f16v{};
  u4 A0[DEPTH], A1[DEPTH], B0[DEPTH], B1[DEPTH];
#pragma unroll
  for (int s = 0; s < DEPTH; s++) { A0[s] = w0[s * 64]; B0[s] = h0[s * 64]; A1[s] = w1[s * 64]; B1[s] = h1[s * 64]; }
#pragma unroll
  for (int s = 0; s < KS; s++) {
    const int r = s % DEPTH;
    bf8 a0 = __builtin_bit_cast(bf8, A0[r]), a1 = __builtin_bit_cast(bf8, A1[r]);
    bf8 b0 = __builtin_bit_cast(bf8, B0[r]), b1 = __builtin_bit_cast(bf8, B1[r]);
    acc[0] = __builtin_amdgcn_mfma_f32_32x32x16_bf16(a0, b0, acc[0], 0, 0, 0);
    acc[2] = __builtin_amdgcn_mfma_f32_32x32x16_bf16(a1, b0, acc[2], 0, 0, 0);
    acc[1] = __builtin_amdgcn_mfma_f32_32x32x16_bf16(a0, b1, acc[1], 0, 0, 0);
    acc[3] = __builtin_amdgcn_mfma_f32_32x32x16_bf16(a1, b1, acc[3], 0, 0, 0);
    if (s + DEPTH < KS) { A0[r] = w0[(s + DEPTH) * 64]; B0[r] = h0[(s + DEPTH) * 64]; A1[r] = w1[(s + DEPTH) * 64]; B1[r] = h1[(s + DEPTH) * 64]; }
    __builtin_amdgcn_sched_barrier(0);
  }
  ST(1);
  const u32 red = sm0 + 8192;
  f16v mine;
#pragma unroll
  for (int k = 0; k < 4; k++) {
    if (k == w) { mine = acc[k]; }
    else {
      const int slot = (w - k - 1) & 3;
      const u32 a = red + (k * 3 + slot) * 4096 + lane * 16;
#pragma unroll
      for (int e = 0; e < 4; e++) *(LAS f4*)(a + e * 1024) = f4{acc[k][4 * e], acc[k][4 * e + 1], acc[k][4 * e + 2], acc[k][4 * e + 3]};
    }
  }
  ST(2);
  WAIT_VM(0);
  WAIT_LGKM0();
  BAR();
  ST(3);
  {
    const u32 a = red + w * 3 * 4096 + lane * 16;
    const int G = lane >> 4, qq = (lane & 15) >> 2, p = lane & 3;
    const u32 ya = sm0 + (qc * 32 + 4 * (G >> 1) + qq) * 128 + (qt * 32 + 16 * (G & 1) + 4 * p) * 2;
    u2 o[4];
#pragma unroll
    for (int e = 0; e < 4; e++) {
      f4 v = *(LAS f4*)(a + e * 1024) + *(LAS f4*)(a + 4096 + e * 1024) + *(LAS f4*)(a + 8192 + e * 1024);
      u2 yv = lds_tr(ya + e * 8 * 128);
      float r0 = mine[4 * e] + v[0] + (blo(xv[e][0]) + blo(yv[0]));
      float r1 = mine[4 * e + 1] + v[1] + (bhi(xv[e][0]) + bhi(yv[0]));
      float r2 = mine[4 * e + 2] + v[2] + (blo(xv[e][1]) + blo(yv[1]));
      float r3 = mine[4 * e + 3] + v[3] + (bhi(xv[e][1]) + bhi(yv[1]));
      o[e] = u2{pk_bf16(r0, r1), pk_bf16(r2, r3)};
    }
#pragma unroll
    for (int e = 0; e < 4; e++) *(u2*)(out + (size_t)tok * D + cb * 64 + qc * 32 + 8 * e + 4 * h) = o[e];
  }
  ST(4); WAIT_VM(0); ST(5); ST_OUT(2);
}

extern "C" void kernel_launch(void* const* d_in, const int* in_sizes, int n_in, void* d_out, int out_size,
                              void* d_ws, size_t ws_size, hipStream_t stream) {
  const u16* x = (const u16*)d_in[0];
  char* ws = (char*)d_ws;
  hipFuncSetAttribute((const void*)k1, hipFuncAttributeMaxDynamicSharedMemorySize, 4 * RW);
  hipFuncSetAttribute((const void*)k2, hipFuncAttributeMaxDynamicSharedMemorySize, K2_LDS);
  k1<<<256, 256, 4 * RW, stream>>>(x, (const u16*)d_in[8], (const u16*)d_in[9], (const u16*)d_in[10], ws);
  k2<<<256, 256, K2_LDS, stream>>>((const u16*)d_in[1], (const u16*)d_in[2], (const u16*)d_in[3], (const u16*)d_in[4],
                                   (const u16*)d_in[5], (const u16*)d_in[6], ws);
  k3<<<256, 256, K3_LDS, stream>>>(x, ws, (u16*)d_out);
}
```

```cpp
#include <hip/hip_runtime.h>
#include <hip/hip_bf16.h>

typedef unsigned short u16;
typedef unsigned int u32;
typedef unsigned long long u64;
typedef __attribute__((ext_vector_type(8))) __bf16 bf8;
typedef __attribute__((ext_vector_type(2))) __bf16 bf2;
typedef __attribute__((ext_vector_type(8))) _Float16 h8;
typedef __attribute__((ext_vector_type(2))) _Float16 h2;
typedef __attribute__((ext_vector_type(4))) u32 u4;
typedef __attribute__((ext_vector_type(2))) u32 u2;
typedef __attribute__((ext_vector_type(4))) short s4;
typedef __attribute__((ext_vector_type(16))) float f16v;
typedef __attribute__((ext_vector_type(4))) float f4;
typedef __attribute__((ext_vector_type(2))) float f2;
#define LAS __attribute__((address_space(3)))
#define GAS __attribute__((address_space(1)))
#define DEV __device__ __forceinline__

#define KROT 0

constexpr int D = 512, NM = 128;
constexpr u32 WS_H = 0;
constexpr u32 WS_WD = WS_H + 64 * 112 * 1024;
constexpr u32 WS_XT = WS_WD + 32 * 16 * 2048;
constexpr u32 WS_YT = WS_XT + 2 * 1024 * 1024;
constexpr u32 WS_SS = WS_YT + 2 * 1024 * 1024;
constexpr u32 WS_CS = WS_SS + 2048 * 8;
constexpr u32 WS_FX = WS_CS + 14 * 4096;
constexpr u32 WS_FW = WS_FX + 128 * 8;
constexpr u32 WS_FC = WS_FW + 128 * 8;
constexpr u32 WS_FH = WS_FC + 128 * 8;
constexpr u32 WS_FY = WS_FH + 32 * 32 * 8;
constexpr u32 WS_EP = WS_FY + 2 * 512 * 8;
constexpr u32 WS_END = WS_EP + 256;

constexpr int L_WR = 0;
constexpr int L_XR = 65536;
constexpr int L_KS = 114688;
constexpr int KSZ = 8704 + 2688;
constexpr int L_CS = L_KS + 4 * KSZ;
constexpr int L_MS = L_CS + 1024;
constexpr int L_MI = L_MS + 512;
constexpr int LDS_BYTES = L_MI + 64;
constexpr int HREG = 26624;
constexpr int NWR = 15;
constexpr int SPIN_CAP = 1 << 17;

DEV float blo(u32 v) { return __uint_as_float(v << 16); }
DEV float bhi(u32 v) { return __uint_as_float(v & 0xffff0000u); }
DEV u32 pk_bf16(float a, float b) { f2 v = {a, b}; return __builtin_bit_cast(u32, __builtin_convertvector(v, bf2)); }
DEV u32 pk_f16(float a, float b) { f2 v = {a, b}; return __builtin_bit_cast(u32, __builtin_convertvector(v, h2)); }
DEV bf2 as_bf2(u32 v) { return __builtin_bit_cast(bf2, v); }
DEV h2 as_h2(u32 v) { return __builtin_bit_cast(h2, v); }
DEV float rbf(float f) { return blo(pk_bf16(f, f)); }

DEV void dma16(u32 lds, const void* sbase, u32 voff) {
  asm volatile("s_mov_b32 m0, %0\n\ts_nop 0\n\tglobal_load_lds_dwordx4 %1, %2" ::"s"(lds), "v"(voff), "s"(sbase) : "memory");
}
template <int N> DEV void wait_vm() { asm volatile("s_waitcnt vmcnt(%0)" ::"n"(N) : "memory"); }
#define WAIT_LGKM0() asm volatile("s_waitcnt lgkmcnt(0)" ::: "memory")
#define SB() __builtin_amdgcn_sched_barrier(0)
#define BAR() do { SB(); asm volatile("s_barrier" ::: "memory"); SB(); } while (0)

#define ST_DECL
#define ST(i)
#define ST_OUT()

template <int V> struct ic { static constexpr int value = V; };
template <int A, int B, class Fn> DEV void static_for(Fn&& f) {
  if constexpr (A < B) { f(ic<A>{}); static_for<A + 1, B>(f); }
}
template <class T> DEV T lds_ld(u32 a) { return *(LAS T*)(size_t)a; }
template <class T> DEV void lds_st(u32 a, T v) { *(LAS T*)(size_t)a = v; }
DEV u2 lds_tr(u32 a) { return __builtin_bit_cast(u2, __builtin_amdgcn_ds_read_tr16_b64_v4i16((LAS s4*)(size_t)a)); }
DEV u4 cat(u2 a, u2 b) { return u4{a[0], a[1], b[0], b[1]}; }

typedef __amdgpu_buffer_rsrc_t rsrc_t;
DEV u4 ld128_dev(rsrc_t r, u32 v, u32 s) { return __builtin_amdgcn_raw_buffer_load_b128(r, v, s, 16); }
DEV u2 ld64_dev(rsrc_t r, u32 v, u32 s) { return __builtin_amdgcn_raw_buffer_load_b64(r, v, s, 16); }
DEV void st128_dev(rsrc_t r, u32 v, u4 d) { __builtin_amdgcn_raw_buffer_store_b128(d, r, v, 0, 16); }
DEV void st64_dev(rsrc_t r, u32 v, u2 d) { __builtin_amdgcn_raw_buffer_store_b64(d, r, v, 0, 16); }
DEV void flag_store(char* ws, u32 off, u64 tag) {
  __hip_atomic_store((GAS u64*)(ws + off), tag, __ATOMIC_RELAXED, __HIP_MEMORY_SCOPE_AGENT);
}
DEV u64 flag_load(const char* ws, u32 off) {
  return __hip_atomic_load((GAS u64*)(ws + off), __ATOMIC_RELAXED, __HIP_MEMORY_SCOPE_AGENT);
}
template <int SLP> DEV void flag_wait(const char* ws, u32 off, u64 tag) {
#pragma unroll 1
  for (int s = 0; s < SPIN_CAP; s++) {
    if (__all(flag_load(ws, off) == tag)) break;
    __builtin_amdgcn_s_sleep(SLP);
  }
  asm volatile("" ::: "memory");
}
DEV void flag_peek(u64& v, const char* ws, u32 off) {
  asm volatile("global_load_dwordx2 %0, %1, %2 sc1" : "=v"(v) : "v"(off), "s"(ws) : "memory");
}

DEV float xsum16(float v) { u32 u = __float_as_uint(v); auto r = __builtin_amdgcn_permlane16_swap(u, u, false, false); return __uint_as_float(r[0]) + __uint_as_float(r[1]); }
DEV float xsum32(float v) { u32 u = __float_as_uint(v); auto r = __builtin_amdgcn_permlane32_swap(u, u, false, false); return __uint_as_float(r[0]) + __uint_as_float(r[1]); }
DEV u32 xmax16(u32 u) { auto r = __builtin_amdgcn_permlane16_swap(u, u, false, false); return max(r[0], r[1]); }
DEV u32 xmax32(u32 u) { auto r = __builtin_amdgcn_permlane32_swap(u, u, false, false); return max(r[0], r[1]); }

DEV f2 cmul_pk(f2 a, f2 b) {
  f2 t;
  asm("v_pk_mul_f32 %0, %1, %2 op_sel_hi:[0,1]\n\tv_pk_fma_f32 %0, %1, %2, %0 op_sel:[1,1,0] op_sel_hi:[1,0,1] neg_lo:[0,1,0]"
      : "=&v"(t) : "v"(a), "v"(b));
  return t;
}
struct cf { float r, i; };
DEV cf cmul(cf a, cf b) {
  cf t;
  asm("v_mul_f32 %0, %2, %4\n\tv_mul_f32 %1, %2, %5\n\tv_fma_f32 %0, -%3, %5, %0\n\tv_fma_f32 %1, %3, %4, %1"
      : "=&v"(t.r), "=&v"(t.i) : "v"(a.r), "v"(a.i), "v"(b.r), "v"(b.i));
  return t;
}
DEV float mul1(float a, float b) { float t; asm("v_mul_f32 %0, %1, %2" : "=v"(t) : "v"(a), "v"(b)); return t; }
DEV float add1(float a, float b) { float t; asm("v_add_f32 %0, %1, %2" : "=v"(t) : "v"(a), "v"(b)); return t; }
DEV cf cexp_at(float lr, float fr, float p) {
  float e = __builtin_amdgcn_exp2f(lr * p);
  float ph = __builtin_amdgcn_fractf(fr * p);
  float c = __builtin_amdgcn_cosf(ph), s = __builtin_amdgcn_sinf(ph);
  asm("s_nop 0" : "+v"(e), "+v"(c), "+v"(s));
  return cf{mul1(e, c), mul1(e, s)};
}
DEV u32 cidx_hi(int l) { return (pk_bf16((float)l, 0.f) & 0xffffu) - 17024u; }

__host__ __device__ constexpr int n_x(int t) { return t == 1 || t == 2 ? 4 : (t >= 3 && t <= 10 ? 2 : 0); }
__host__ __device__ constexpr int n_w(int t) { return t == 0 ? 8 : (t <= 12 ? 4 : 0); }
__host__ __device__ constexpr int n_e(int t) { return t == 7 ? 1 : t == 9 ? 3 : t == 10 ? 4 : t == 11 ? 4 : t == 13 ? 11 : t == 14 ? 8 : 0; }
__host__ __device__ constexpr int n_all(int t) { return n_x(t) + n_w(t) + n_e(t); }
__host__ __device__ constexpr int n_wait(int t) {
  return t == 0 ? 8 : t <= 2 ? 4 + n_all(t) : n_e(t - 2) + n_all(t - 1) + n_all(t);
}

__global__ __launch_bounds__(512, 2) void fused(const u16* __restrict__ x, const u16* __restrict__ lar,
                                                const u16* __restrict__ aim, const u16* __restrict__ cr,
                                                const u16* __restrict__ ci, const u16* __restrict__ ldt,
                                                const u16* __restrict__ dsk, const u16* __restrict__ wg,
                                                const u16* __restrict__ wu, const u16* __restrict__ wd,
                                                char* __restrict__ ws, u16* __restrict__ out) {
  extern __shared__ __attribute__((aligned(16))) char sm[];
  const u32 sm0 = (u32)(size_t)(LAS char*)sm;
  const int b = blockIdx.x, xcd = b & 7, qi = b >> 6, qj = (b >> 3) & 7;
  const bool isS = qj == 7;
  const int ti = (xcd >> 1) * 4 + qi, tj = (xcd & 1) * 7 + qj;
  const int tb = xcd * 4 + qi, cb = qj;
  const int wv = __builtin_amdgcn_readfirstlane(threadIdx.x >> 6);
  const int w = wv & 3;
  const int lane = threadIdx.x & 63;
  const int G = lane >> 4, qq = (lane & 15) >> 2, p = lane & 3, a16 = lane & 15;
  const int chl = w >> 1, hw = w & 1, ch = b * 2 + chl;
  const u32 ks = sm0 + L_KS + w * KSZ;
  const u32 xs = sm0 + L_KS + (2 * chl + 1) * KSZ, Pt = sm0 + L_KS + 2 * chl * KSZ + 64;
  const rsrc_t rs = __builtin_amdgcn_make_buffer_rsrc(ws, 0, WS_END, 0x00020000);
  auto scr = [&](int v) -> u32 { return sm0 + ((v & 1) ? L_KS + v * KSZ + 4608 : 4 * HREG + (v >> 1) * 4096); };
  ST_DECL ST(0);

  if (wv < 4) {
    u32 p_lar, p_aim, p_ldt, p_dsk;
    u64 ep;
    {
      const u32 o1 = (ch * NM + hw * 64 + lane) * 2, o3 = ch * 2;
      asm volatile("global_load_ushort %0, %5, %8\n\tglobal_load_ushort %1, %5, %9\n\tglobal_load_ushort %2, %6, %10\n\t"
                   "global_load_ushort %3, %6, %11\n\tglobal_load_dwordx2 %4, %7, %12 sc1"
                   : "=&v"(p_lar), "=&v"(p_aim), "=&v"(p_ldt), "=&v"(p_dsk), "=&v"(ep)
                   : "v"(o1), "v"(o3), "v"(WS_EP), "s"(lar), "s"(aim), "s"(ldt), "s"(dsk), "s"(ws) : "memory");
    }
    if (lane == 0) lds_st<u32>(sm0 + L_MI + 32 + w * 4, 0u);
    const u32 sreg = sm0 + w * 28672;
    if (isS) {
      const char* src = (const char*)wd + (size_t)((tb & 3) * 28 + 4 * w) * 16384 + (tb >> 2) * 128;
      const u32 vo = (lane >> 3) * 1024 + (lane & 7) * 16;
#pragma unroll
      for (int i = 0; i < 8; i++) dma16(sreg + 16640 + i * 1024, src + i * 8192, vo);
      const char* xb = (const char*)x + (size_t)(tb * 64 + w * 16) * 1024;
      const u32 ve = (lane >> 3) * 1024 + (((lane & 7) ^ (lane >> 4)) << 4);
      const u32 vd = (8 + (lane >> 3)) * 1024 + (((lane & 7) ^ (4 + (lane >> 4))) << 4);
#pragma unroll
      for (int c = 0; c < 8; c++) { dma16(sreg + c * 2048, xb + c * 128, ve); dma16(sreg + c * 2048 + 1024, xb + c * 128, vd); }
      wait_vm<24>();
    } else {
      wait_vm<0>();
    }
    SB();
    asm volatile("" : "+v"(p_lar), "+v"(p_aim), "+v"(p_ldt), "+v"(p_dsk), "+v"(ep) :: "memory");
    ST(1);
    const float v_dsk = blo(p_dsk);
    if (w == 0 && lane == 0) lds_st<u64>(sm0 + L_MI, ep + 1);
    {
      const float dt = rbf(__expf(blo(p_ldt)));
      const float lr = -rbf(__expf(blo(p_lar))) * dt * 1.44269504088896f;
      const float fr = blo(p_aim) * dt * 0.15915494309189535f;
      const u32 wa = ks + lane * 4;
      auto putV = [&](int col, f2 v) { lds_st<u32>(wa + col * 272, pk_f16(v[0], v[1])); };
      cf c8 = cexp_at(lr, fr, 8.f), c64 = cexp_at(lr, fr, 64.f);
      f2 v[16];
      v[0] = f2{1.f, 0.f}; v[1] = f2{c8.r, c8.i}; v[8] = f2{c64.r, c64.i};
      v[2] = cmul_pk(v[1], v[1]);
      const f2 e128 = cmul_pk(v[8], v[8]);
      v[3] = cmul_pk(v[1], v[2]); v[4] = cmul_pk(v[2], v[2]);
#pragma unroll
      for (int j = 1; j < 4; j++) v[4 + j] = cmul_pk(v[j], v[4]);
#pragma unroll
      for (int j = 1; j < 8; j++) v[8 + j] = cmul_pk(v[j], v[8]);
#pragma unroll
      for (int j = 0; j < 16; j++) putV(j, v[j]);
      putV(16, e128);
#pragma unroll
      for (int j = 1; j < 16; j++) putV(16 + j, cmul_pk(v[j], e128));
    }
    WAIT_LGKM0();
    ST(2);
    BAR();
    ST(3);
    const u64 tag = lds_ld<u64>(sm0 + L_MI);
    if (!isS) {
      const int th = w >> 1, fh = w & 1;
      f4 acc[8][4];
#pragma unroll
      for (int n = 0; n < 8; n++)
#pragma unroll
        for (int j = 0; j < 4; j++) acc[n][j] = f4{};
      {
        u32 ta[4];
#pragma unroll
        for (int cc = 0; cc < 4; cc++) ta[cc] = sm0 + L_WR + (8 * G + qq) * 256 + (((4 * fh + cc) ^ (qq | ((G & 1) << 2))) << 5) + 8 * p;
        const u32 xl = sm0 + L_XR + (th * 64 + a16) * 128;
        const u32 xa[2] = {xl + ((G ^ (a16 >> 1)) << 4), xl + (((4 + G) ^ (a16 >> 1)) << 4)};
        auto wadr = [&](int t, int n) -> u32 { return ta[n >> 1] + (t & 3) * 16384 + (n & 1) * 8192; };
        auto xadr = [&](int t, int j) -> u32 { return xa[t & 1] + ((t >> 1) % 3) * 16384 + j * 2048; };
        u4 wq[4], xf[4], yf[4];
#pragma unroll
        for (int j = 0; j < 4; j++) xf[j] = lds_ld<u4>(xadr(0, j));
#pragma unroll
        for (int n = 0; n < 3; n++) wq[n] = cat(lds_tr(wadr(0, n)), lds_tr(wadr(0, n) + 1024));
#pragma unroll
        for (int t = 0; t < 16; t++) {
          if (t == 4) ST(4);
          if (t == 8) ST(5);
          if (t == 12) ST(6);
#pragma unroll
          for (int n = 0; n < 8; n++) {
            const bf8 a = __builtin_bit_cast(bf8, wq[n & 3]);
            const bool r1 = n < 5 || (n >= 6 && t < 15), r2 = n == 6 && t < 15;
            const u32 a1 = n < 5 ? wadr(t, n + 3) : wadr(t + 1, n == 6 ? 0 : 2), a2 = wadr(t + 1, 1);
            u2 lo, hi, lo2, hi2;
            acc[n][0] = __builtin_amdgcn_mfma_f32_16x16x32_bf16(a, __builtin_bit_cast(bf8, xf[0]), acc[n][0], 0, 0, 0);
            if (r1) lo = lds_tr(a1);
            SB();
            acc[n][1] = __builtin_amdgcn_mfma_f32_16x16x32_bf16(a, __builtin_bit_cast(bf8, xf[1]), acc[n][1], 0, 0, 0);
            if (r1) hi = lds_tr(a1 + 1024);
            SB();
            acc[n][2] = __builtin_amdgcn_mfma_f32_16x16x32_bf16(a, __builtin_bit_cast(bf8, xf[2]), acc[n][2], 0, 0, 0);
            if (n < 4 && t < 15) yf[n] = lds_ld<u4>(xadr(t + 1, n));
            if (r2) lo2 = lds_tr(a2);
            SB();
            acc[n][3] = __builtin_amdgcn_mfma_f32_16x16x32_bf16(a, __builtin_bit_cast(bf8, xf[3]), acc[n][3], 0, 0, 0);
            if (r2) hi2 = lds_tr(a2 + 1024);
            if (r1) wq[n < 5 ? ((n + 3) & 3) : (n == 6 ? 0 : 2)] = cat(lo, hi);
            if (r2) wq[1] = cat(lo2, hi2);
            SB();
            if (n == 5 && t < 15) { WAIT_LGKM0(); BAR(); }
          }
#pragma unroll
          for (int j = 0; j < 4; j++) xf[j] = yf[j];
        }
      }
      ST(7);
      BAR();
      float nm[4];
#pragma unroll
      for (int j = 0; j < 4; j++) nm[j] = -lds_ld<float>(sm0 + L_MS + (th * 64 + j * 16 + a16) * 4);
      const u32 hs = WS_H + ((ti * 4 + th * 2) * 112 + tj * 8 + fh * 4) * 1024 + lane * 16;
#pragma unroll
      for (int cc = 0; cc < 4; cc++) {
        const f4 cg = lds_ld<f4>(sm0 + L_CS + (fh * 64 + cc * 16 + 4 * G) * 4), cu = lds_ld<f4>(sm0 + L_CS + 512 + (fh * 64 + cc * 16 + 4 * G) * 4);
#pragma unroll
        for (int pp = 0; pp < 2; pp++) {
          u2 hv[2];
#pragma unroll
          for (int z = 0; z < 2; z++) {
            const int j = 2 * pp + z;
            const f4 pr = (acc[2 * cc][j] + cg * nm[j]) * (acc[2 * cc + 1][j] + cu * nm[j]);
            hv[z] = u2{pk_bf16(pr[0], pr[1]), pk_bf16(pr[2], pr[3])};
          }
          auto r0 = __builtin_amdgcn_permlane16_swap(hv[0][0], hv[1][0], false, false);
          auto r1 = __builtin_amdgcn_permlane16_swap(hv[0][1], hv[1][1], false, false);
          st128_dev(rs, hs + (pp * 112 + cc) * 1024, u4{r0[0], r1[0], r0[1], r1[1]});
        }
      }
    } else {
      wait_vm<16>();
      {
        const u32 ra = sreg + 16640 + (8 * (G >> 1) + qq) * 128 + 32 * (G & 1) + 8 * p;
#pragma unroll
        for (int i = 0; i < 4; i++)
#pragma unroll
          for (int e = 0; e < 2; e++)
            st128_dev(rs, WS_WD + ((tb * 16 + 4 * w + i) * 2 + e) * 1024 + lane * 16,
                      cat(lds_tr(ra + i * 2048 + e * 64), lds_tr(ra + i * 2048 + e * 64 + 512)));
      }
      wait_vm<8>();
      u4 xr[16];
      const u32 xa0 = sreg + a16 * 128 + ((G ^ (a16 >> 1)) << 4), xa1 = sreg + a16 * 128 + (((4 + G) ^ (a16 >> 1)) << 4);
#pragma unroll
      for (int k = 0; k < 16; k++) xr[k] = lds_ld<u4>(((k & 1) ? xa1 : xa0) + (k >> 1) * 2048);
      float s1 = 0.f, s2 = 0.f;
      const bf2 ones = as_bf2(0x3f803f80u);
#pragma unroll
      for (int k = 0; k < 16; k++)
#pragma unroll
        for (int e = 0; e < 4; e++) {
          bf2 v = as_bf2(xr[k][e]);
          s1 = __builtin_amdgcn_fdot2_f32_bf16(v, ones, s1, false);
          s2 = __builtin_amdgcn_fdot2_f32_bf16(v, v, s2, false);
        }
      const float mean = xsum32(xsum16(s1)) * (1.f / D);
      const float var = fmaxf(xsum32(xsum16(s2)) * (1.f / D) - mean * mean, 0.f);
      const float sd = rbf(__builtin_amdgcn_sqrtf(rbf(rbf(var) + 1e-5f)));
      const float rsd = __builtin_amdgcn_rcpf(sd), mb = rbf(mean), rc = -mb * rsd;
      wait_vm<0>();
      if (lane == 0) flag_store(ws, WS_FW + (tb * 4 + w) * 8, tag);
      if (lane < 16) st64_dev(rs, WS_SS + (tb * 64 + w * 16 + lane) * 8, u2{__float_as_uint(mb), __float_as_uint(rsd * rsd)});
      WAIT_LGKM0();
#pragma unroll
      for (int k = 0; k < 16; k++) {
        u4 o;
#pragma unroll
        for (int e = 0; e < 4; e++) o[e] = pk_f16(__builtin_fmaf(blo(xr[k][e]), rsd, rc), __builtin_fmaf(bhi(xr[k][e]), rsd, rc));
        lds_st<u4>(sreg + a16 * 1040 + k * 64 + G * 16, o);
      }
      const u32 ra = sreg + (8 * (G & 1) + qq) * 1040 + 32 * (G >> 1) + 8 * p;
      const u32 xo = WS_XT + ((4 * (G >> 1) + (a16 >> 2)) * 128 + tb * 4 + w) * 128 + (a16 & 3) * 32 + 16 * (G & 1);
#pragma unroll
      for (int n = 0; n < 16; n++) st128_dev(rs, xo + n * (8 * 128 * 128), cat(lds_tr(ra + n * 64), lds_tr(ra + n * 64 + 4160)));
      wait_vm<0>();
      if (lane == 0) flag_store(ws, WS_FX + (tb * 4 + w) * 8, tag);
      ST(6);
      BAR();
      ST(7);
      BAR();
    }
    BAR();
    ST(8);
    const int qt = w & 1, qc = w >> 1;
    u2 xv[4];
    f4 cac[8];
#pragma unroll
    for (int j = 0; j < 8; j++) cac[j] = f4{};
    {
      const u32 ba = xs + 2 * (240 + 16 * a16 + 8 * G - 16) - 256 * hw - 192;
      const u32 pa = Pt + 4 * (a16 - 8 * G + 16 - 6) + 512 * hw;
      u4 bf[2][8], tf[2][8];
      auto ldB = [&](int r, int i) { return lds_ld<u4>(ba + 192 - 64 * r + 512 * i); };
      auto ldT = [&](int r, int k) { const u32 z = pa + 128 * r + 1024 * k;
                                     return u4{lds_ld<u32>(z + 24), lds_ld<u32>(z + 16), lds_ld<u32>(z + 8), lds_ld<u32>(z)}; };
      tf[0][0] = ldT(0, 0);
#pragma unroll
      for (int i = 0; i < 8; i++) bf[0][i] = ldB(0, i);
#pragma unroll
      for (int k = 1; k < 8; k++) tf[0][k] = ldT(0, k);
      SB();
#pragma unroll
      for (int r = 0; r < 4; r++) {
        if (r < 3) {
          tf[(r + 1) & 1][0] = ldT(r + 1, 0);
#pragma unroll
          for (int i = 0; i < 8; i++) bf[(r + 1) & 1][i] = ldB(r + 1, i);
#pragma unroll
          for (int k = 1; k < 8; k++) tf[(r + 1) & 1][k] = ldT(r + 1, k);
        }
#pragma unroll
        for (int k = 0; k < 8; k++)
#pragma unroll
          for (int j = k; j < 8; j++)
            cac[j] = __builtin_amdgcn_mfma_f32_16x16x32_f16(__builtin_bit_cast(h8, tf[r & 1][k]), __builtin_bit_cast(h8, bf[r & 1][j - k]), cac[j], 0, 0, 0);
        if (r < 3) {
#pragma unroll
          for (int z = 0; z < 12; z++) {
            __builtin_amdgcn_sched_group_barrier(0x8, 1, 0);
            __builtin_amdgcn_sched_group_barrier(0x100, 1, 0);
            __builtin_amdgcn_sched_group_barrier(0x8, 1, 0);
            __builtin_amdgcn_sched_group_barrier(0x100, 1, 0);
            __builtin_amdgcn_sched_group_barrier(0x8, 1, 0);
          }
        }
        SB();
        if (r == 0) {
          wait_vm<0>();
          if (!isS && lane == 0) flag_store(ws, WS_FH + ((ti * 2 + (w >> 1)) * 32 + tj * 2 + (w & 1)) * 8, tag);
          const u16* xp = x + (size_t)(tb * 64 + qt * 32 + (lane & 31)) * D + cb * 64 + qc * 32 + 4 * (lane >> 5);
#pragma unroll
          for (int e = 0; e < 4; e++) xv[e] = *(const u2*)(xp + 8 * e);
          ST(9);
          SB();
        }
      }
    }
    ST(10);
    const u32 mine = scr(w), oth = scr(w ^ 1);
    {
      f4 keep[4];
#pragma unroll
      for (int j = 0; j < 4; j++) {
        f4 snd = hw ? cac[j] : cac[4 + j];
        keep[j] = hw ? cac[4 + j] : cac[j];
        lds_st<f4>(mine + j * 1024 + lane * 16, snd);
      }
      const float kscale = lds_ld<float>(sm0 + L_MI + 16 + chl * 4);
      WAIT_LGKM0();
      if (lane == 0) *(volatile LAS u32*)(size_t)(sm0 + L_MI + 32 + w * 4) = 1u;
#pragma unroll 1
      for (int s = 0; s < SPIN_CAP; s++) {
        if (*(volatile LAS u32*)(size_t)(sm0 + L_MI + 32 + (w ^ 1) * 4) != 0u) break;
      }
      asm volatile("" ::: "memory");
      u2 o[4];
#pragma unroll
      for (int j = 0; j < 4; j++) {
        f4 v = keep[j] + lds_ld<f4>(oth + j * 1024 + lane * 16);
        u2 xn2 = lds_ld<u2>(xs + 480 + 2 * (1024 * hw + 256 * j + 16 * a16 + 4 * G));
        h2 x01 = as_h2(xn2[0]), x23 = as_h2(xn2[1]);
        o[j][0] = pk_bf16(v[0] * kscale + v_dsk * (float)x01[0], v[1] * kscale + v_dsk * (float)x01[1]);
        o[j][1] = pk_bf16(v[2] * kscale + v_dsk * (float)x23[0], v[3] * kscale + v_dsk * (float)x23[1]);
      }
      const u32 yo = WS_YT + ((16 * hw + 4 * (G & 1) + (a16 >> 2)) * 512 + ch) * 128 + (a16 & 3) * 32 + (G >> 1) * 16;
#pragma unroll
      for (int j = 0; j < 4; j += 2) {
        auto r0 = __builtin_amdgcn_permlane16_swap(o[j][0], o[j + 1][0], false, false);
        auto r1 = __builtin_amdgcn_permlane16_swap(o[j][1], o[j + 1][1], false, false);
        st128_dev(rs, yo + j * (4 * 512 * 128), u4{r0[0], r1[0], r0[1], r1[1]});
      }
      wait_vm<0>();
      if (lane == 0) flag_store(ws, WS_FY + (hw * 512 + ch) * 8, tag);
    }
    ST(11);
    flag_wait<4>(ws, WS_FY + ((tb >> 4) * 512 + cb * 64 + qc * 32 + (lane & 31)) * 8, tag);
    ST(12);
    {
      const u32 ys = WS_YT + (tb * 512 + cb * 64 + qc * 32) * 128 + qt * 64;
      const u32 vo = (lane >> 2) * 128 + (lane & 3) * 16;
      u4 y0 = ld128_dev(rs, vo, ys), y1 = ld128_dev(rs, vo, ys + 2048);
      lds_st<u4>(oth + lane * 16, y0);
      lds_st<u4>(oth + 1024 + lane * 16, y1);
      const u32 ya = oth + (4 * (G >> 1) + qq) * 64 + 32 * (G & 1) + 8 * p;
      u2 yv[4];
#pragma unroll
      for (int e = 0; e < 4; e++) yv[e] = lds_tr(ya + e * 512);
      WAIT_LGKM0();
#pragma unroll
      for (int e = 0; e < 4; e++)
        lds_st<f4>(oth + e * 1024 + lane * 16,
                   f4{blo(xv[e][0]) + blo(yv[e][0]), bhi(xv[e][0]) + bhi(yv[e][0]), blo(xv[e][1]) + blo(yv[e][1]), bhi(xv[e][1]) + bhi(yv[e][1])});
    }
    WAIT_LGKM0();
    ST(13);
    BAR();
    if (b == 0 && w == 0) {
#pragma unroll 1
      for (int k = 0; k < 4; k++) flag_wait<1>(ws, WS_FY + (k * 64 + lane) * 16, tag);
      if (lane == 0) flag_store(ws, WS_EP, tag);
    }
  } else {
    u32 p_lar, p_aim, p_cr, p_ci, p_cr2, p_ci2, p_ldt;
    auto ld_params = [&]() {
      const u32 o1 = (ch * NM + hw * 64 + lane) * 2, o2 = (ch * NM + (hw ^ 1) * 64 + lane) * 2, o3 = ch * 2;
      asm volatile("global_load_ushort %0, %7, %10\n\tglobal_load_ushort %1, %7, %11\n\tglobal_load_ushort %2, %7, %12\n\t"
                   "global_load_ushort %3, %7, %13\n\tglobal_load_ushort %4, %8, %12\n\tglobal_load_ushort %5, %8, %13\n\t"
                   "global_load_ushort %6, %9, %14"
                   : "=&v"(p_lar), "=&v"(p_aim), "=&v"(p_cr), "=&v"(p_ci), "=&v"(p_cr2), "=&v"(p_ci2), "=&v"(p_ldt)
                   : "v"(o1), "v"(o2), "v"(o3), "s"(lar), "s"(aim), "s"(cr), "s"(ci), "s"(ldt) : "memory");
    };
    u4 vb[8];
    float lr, fr;
    cf e1, e2, e4, e256, e512, e1024, u[8], t0, t1;
    const u32 fra = ks + (lane & 31) * 272 + (lane >> 5) * 16;
    const u32 wa = ks + lane * 4;
    auto putU = [&](int row, cf v) { lds_st<u32>(wa + row * 272, pk_f16(v.r, v.i)); };
    auto kpiece = [&](int i) {
      if (i == 0) {
#pragma unroll
        for (int s = 0; s < 8; s++) vb[s] = lds_ld<u4>(fra + s * 32);
        const float dt = rbf(__expf(blo(p_ldt)));
        lr = mul1(mul1(-rbf(__expf(blo(p_lar))), dt), 1.44269504088896f);
        fr = mul1(mul1(blo(p_aim), dt), -0.15915494309189535f);
        u32 cm = max(max(p_cr & 0x7fffu, p_ci & 0x7fffu), max(p_cr2 & 0x7fffu, p_ci2 & 0x7fffu));
        cm = max(cm, (u32)__builtin_amdgcn_update_dpp(0, (int)cm, 0xB1, 0xf, 0xf, true));
        cm = max(cm, (u32)__builtin_amdgcn_update_dpp(0, (int)cm, 0x4E, 0xf, 0xf, true));
        cm = max(cm, (u32)__builtin_amdgcn_update_dpp(0, (int)cm, 0x141, 0xf, 0xf, true));
        cm = max(cm, (u32)__builtin_amdgcn_update_dpp(0, (int)cm, 0x140, 0xf, 0xf, true));
        cm = xmax32(xmax16(cm));
        const u32 eb = cm >> 7;
        const float inv = __uint_as_float((254u - eb) << 23);
        if (lane == 0) lds_st<float>(sm0 + L_MI + 16 + chl * 4, 2.f * dt * __uint_as_float(eb << 23));
        u[0] = cf{mul1(blo(p_cr), inv), mul1(-blo(p_ci), inv)};
      } else if (i == 1) {
        e1 = cexp_at(lr, fr, 1.f); e256 = cexp_at(lr, fr, 256.f);
      } else if (i == 2) {
        e1024 = cexp_at(lr, fr, 1024.f);
        e2 = cmul(e1, e1); u[1] = cmul(u[0], e1); e512 = cmul(e256, e256);
        putU(0, u[0]); putU(1, u[1]);
      } else if (i == 3) {
        e4 = cmul(e2, e2); u[2] = cmul(u[0], e2); u[3] = cmul(u[1], e2);
        putU(2, u[2]); putU(3, u[3]);
        u[4] = cmul(u[0], e4); putU(4, u[4]); putU(5, cmul(u[1], e4));
      } else if (i == 4) {
        u[6] = cmul(u[2], e4); putU(6, u[6]); putU(7, cmul(u[3], e4));
        putU(8, cmul(u[0], e256)); putU(9, cmul(u[2], e256));
      } else if (i == 5) {
        putU(10, cmul(u[4], e256)); putU(11, cmul(u[6], e256));
        t0 = cmul(u[0], e512); t1 = cmul(u[4], e512);
        putU(12, t0); putU(13, t1);
      } else if (i == 6) {
        putU(14, cmul(t0, e256)); putU(15, cmul(t1, e256));
        t0 = cmul(u[0], e1024); putU(16, t0); putU(17, cmul(t0, e256));
      } else if (i == 7) {
        t1 = cmul(t0, e512); putU(18, t1); putU(19, cmul(t1, e256)); putU(20, cmul(t0, e1024));
      } else if (i == 8) {
        f16v d = f16v{};
#pragma unroll
        for (int s = 0; s < 8; s++)
          d = __builtin_amdgcn_mfma_f32_32x32x16_f16(__builtin_bit_cast(h8, lds_ld<u4>(fra + s * 32)), __builtin_bit_cast(h8, vb[s]), d, 0, 0, 0);
        const u32 ct = ks + 8704;
        const int j = lane & 31, h = lane >> 5;
        lds_st<f4>(ct + (8 * j + 4 * h) * 4, f4{d[0], d[1], d[2], d[3]});
        const int i1 = h ? (384 + 2 * j) : (256 + 4 * j), i2 = h ? (448 + 2 * j) : (258 + 4 * j);
        lds_st<f2>(ct + i1 * 4, f2{d[4], d[5]});
        lds_st<f2>(ct + i2 * 4, f2{d[6], d[7]});
        lds_st<float>(ct + (512 + 128 * h + j) * 4, d[8]);
        if (h == 0) { lds_st<float>(ct + (544 + j) * 4, d[9]); lds_st<float>(ct + (576 + j) * 4, d[10]); lds_st<float>(ct + (608 + j) * 4, d[11]); }
      } else {
        const int qd = i - 9;
        const u32 ct = sm0 + L_KS + 2 * chl * KSZ + 8704, pw = Pt + (1024 * hw + 256 * qd + lane) * 4;
        auto rd = [&](u32 a) { return add1(lds_ld<float>(a), lds_ld<float>(a + KSZ)); };
        const int l0 = 1024 * hw + 256 * qd + lane;
        const u32 st = hw ? 32u : (qd == 0 ? 256u : qd == 1 ? 128u : 64u);
        u32 a0, a1, a1f;
        if (qd == 0) {
          a0 = hw ? cidx_hi(l0) : (u32)lane; a1 = hw ? cidx_hi(l0 + 63) : (u32)(lane + 63); a1f = hw ? cidx_hi(l0 - 1) : (u32)(lane ? lane - 1 : 0);
        } else { a0 = cidx_hi(l0); a1 = cidx_hi(l0 + 63); a1f = cidx_hi(l0 - 1); }
        a0 = ct + a0 * 4; a1 = ct + a1 * 4; a1f = ct + a1f * 4;
        float km = rd(a1f);
        if (qd == 0) km = l0 ? km : 0.f;
        lds_st<u32>(pw, pk_f16(rd(a0), km));
#pragma unroll
        for (int z = 1; z < 4; z++) lds_st<u32>(pw + z * 256, pk_f16(rd(a0 + z * st), rd(a1 + (z - 1) * st)));
        if (qd == 0 && !hw) {
          if (lane < 16) lds_st<u32>(Pt - 64 + lane * 4, 0u);
          if (lane < 30) lds_st<u4>(xs + lane * 16, u4{0, 0, 0, 0});
        }
      }
    };
    u4 wr[2 * NWR];
    const u32 wso = WS_WD + (cb * 4 + w) * 32 * 1024;
    const u32 fwo = WS_FW + ((cb * 4 + w) * 4 + (lane & 3)) * 8;
    u64 tag;
    float r2;
    u4 gx0, gx1;
    auto gather_ld = [&]() {
      const u32 so = WS_XT + ((ch >> 2) * 128 + hw * 64) * 128 + (ch & 3) * 32, vo = (lane >> 1) * 128 + (lane & 1) * 16;
      gx0 = ld128_dev(rs, vo, so); gx1 = ld128_dev(rs, vo, so + 4096);
      r2 = __uint_as_float(__builtin_amdgcn_raw_buffer_load_b32(rs, ((w & 1) * 32 + (lane & 31)) * 8 + 4, WS_SS + tb * 512, 16));
    };
    auto gather_st = [&]() {
      lds_st<u4>(xs + 480 + hw * 2048 + lane * 16, gx0);
      lds_st<u4>(xs + 480 + hw * 2048 + 1024 + lane * 16, gx1);
    };
    if (!isS) {
      ld_params();
      const int rot = KROT ? (2 * qi + (xcd >> 2) + qj) & 7 : 0;
      u32 wvo[4];
#pragma unroll
      for (int ii = 0; ii < 4; ii++) wvo[ii] = (4 * ii + G) * 3584 + ((a16 ^ (2 * (G | ((ii >> 1) << 2)))) << 4);
      const char* wb = (const char*)((w & 2) ? wu : wg) + (size_t)(16 * (w & 1)) * 3584 + tj * 256;
      auto dmaW = [&](int t) {
        const char* src = wb + (size_t)((t + 2 * rot) & 15) * (32 * 3584);
#pragma unroll
        for (int ii = 0; ii < 4; ii++) dma16(sm0 + L_WR + (t & 3) * 16384 + w * 4096 + ii * 1024, src, wvo[ii]);
      };
      const u32 ve = (lane >> 3) * 1024 + (((lane & 7) ^ (lane >> 4)) << 4);
      const u32 vd = (8 + (lane >> 3)) * 1024 + (((lane & 7) ^ (4 + (lane >> 4))) << 4);
      const char* xb = (const char*)x + (size_t)(ti * 128 + w * 32) * 1024;
      auto dmaX = [&](int s, int half) {
        const char* src = xb + ((s + rot) & 7) * 128 + half * 16384;
        const u32 d = sm0 + L_XR + (s % 3) * 16384 + w * 4096 + half * 2048;
        dma16(d, src, ve);
        dma16(d + 1024, src, vd);
      };
      dmaX(0, 0); dmaX(0, 1); dmaW(0); dmaW(1); dmaX(1, 0); dmaX(1, 1);
      ST(1);
      asm volatile("s_waitcnt vmcnt(8)" : "+v"(p_lar), "+v"(p_aim), "+v"(p_cr), "+v"(p_ci), "+v"(p_cr2), "+v"(p_ci2), "+v"(p_ldt) :: "memory");
      ST(2);
      BAR();
      tag = lds_ld<u64>(sm0 + L_MI);
      u64 f0, f1, f2_, f3;
      u4 cpart;
      u32 mval;
      const u32 fxo = WS_FX + lane * 8, fco = WS_FC + (tj * 8 + (lane & 7)) * 8;
      static_for<0, 15>([&](auto T) {
        constexpr int t = decltype(T)::value;
        if (t == 1 || t == 2) { dmaX(t + 1, 0); dmaX(t + 1, 1); }
        if (t >= 3 && t <= 10) dmaX((t + 5) >> 1, (t + 5) & 1);
        if (t == 0) { dmaW(2); dmaW(3); }
        else if (t <= 12) dmaW(t + 3);
        if (t == 7) flag_peek(f0, ws, fwo);
        if (t == 9) { flag_peek(f1, ws, fxo); flag_peek(f2_, ws, fxo + 512); flag_peek(f3, ws, fco); }
        if (t == 10) {
          asm volatile("s_waitcnt vmcnt(%1)" : "+v"(f0) : "n"(n_all(8) + n_all(9) + n_x(10) + n_w(10)) : "memory");
          if (!__all(f0 == tag)) flag_wait<1>(ws, fwo, tag);
        }
        if constexpr (t == 10 || t == 11) {
#pragma unroll
          for (int i = 0; i < 4; i++) wr[(t - 10) * 4 + i] = ld128_dev(rs, lane * 16, wso + ((t - 10) * 4 + i) * 1024);
        }
        if (t <= 12) kpiece(t);
        if (t == 13) {
          asm volatile("s_waitcnt vmcnt(%3)" : "+v"(f1), "+v"(f2_), "+v"(f3) : "n"(n_all(11) + n_all(12)) : "memory");
          if (!__all(f1 == tag)) flag_wait<1>(ws, fxo, tag);
          if (!__all(f2_ == tag)) flag_wait<1>(ws, fxo + 512, tag);
          if (!__all(f3 == tag)) flag_wait<1>(ws, fco, tag);
          cpart = ld128_dev(rs, G * 1024 + a16 * 16, WS_CS + tj * 4096 + w * 256);
          mval = __builtin_amdgcn_raw_buffer_load_b32(rs, (lane & 31) * 8, WS_SS + (ti * 128 + w * 32) * 8, 16);
          gather_ld();
#pragma unroll
          for (int i = 8; i < 14; i++) wr[i] = ld128_dev(rs, lane * 16, wso + i * 1024);
        }
        if (t == 14) {
#pragma unroll
          for (int i = 14; i < 22; i++) wr[i] = ld128_dev(rs, lane * 16, wso + i * 1024);
        }
        if (t == 3) ST(3);
        if (t == 8) ST(4);
        if (t == 12) ST(5);
        wait_vm<n_wait(t)>();
        WAIT_LGKM0();
        if (t == 3) ST(16);
        if (t == 8) ST(17);
        if (t == 12) ST(18);
        BAR();
      });
      {
        f4 c4;
#pragma unroll
        for (int e = 0; e < 4; e++) {
          auto r = __builtin_amdgcn_permlane16_swap(cpart[e], cpart[e], false, false);
          const u32 v = __float_as_uint(add1(__uint_as_float(r[0]), __uint_as_float(r[1])));
          auto r2_ = __builtin_amdgcn_permlane32_swap(v, v, false, false);
          c4[e] = add1(__uint_as_float(r2_[0]), __uint_as_float(r2_[1]));
        }
        lds_st<f4>(sm0 + L_CS + w * 256 + a16 * 16, c4);
        lds_st<u32>(sm0 + L_MS + (w * 32 + (lane & 31)) * 4, mval);
        gather_st();
      }
#pragma unroll
      for (int i = 22; i < 2 * NWR; i++) wr[i] = ld128_dev(rs, lane * 16, wso + i * 1024);
      WAIT_LGKM0();
    } else {
      ld_params();
      const int job = tb < 28 ? tb : 27, mat = job >= 14, jt = job - 14 * mat;
      const char* src = (const char*)(mat ? wu : wg) + (size_t)(128 * w + G) * 3584 + jt * 256 + a16 * 16;
      u4 cw[32];
#pragma unroll
      for (int i = 0; i < 32; i++) cw[i] = *(const u4*)(src + (size_t)i * 4 * 3584);
      asm volatile("s_waitcnt vmcnt(32)" : "+v"(p_lar), "+v"(p_aim), "+v"(p_cr), "+v"(p_ci), "+v"(p_cr2), "+v"(p_ci2), "+v"(p_ldt) :: "memory");
      BAR();
      tag = lds_ld<u64>(sm0 + L_MI);
      float cs[8];
#pragma unroll
      for (int e = 0; e < 8; e++) cs[e] = 0.f;
      u32 s0u = 0x00003f80u, s1u = 0x3f800000u;
      asm("" : "+v"(s0u), "+v"(s1u));
      const bf2 sel0 = as_bf2(s0u), sel1 = as_bf2(s1u);
#pragma unroll
      for (int i = 0; i < 32; i++)
#pragma unroll
        for (int e = 0; e < 4; e++) {
          cs[2 * e] = __builtin_amdgcn_fdot2_f32_bf16(as_bf2(cw[i][e]), sel0, cs[2 * e], false);
          cs[2 * e + 1] = __builtin_amdgcn_fdot2_f32_bf16(as_bf2(cw[i][e]), sel1, cs[2 * e + 1], false);
        }
#pragma unroll
      for (int e = 0; e < 8; e++) cs[e] = xsum32(xsum16(cs[e]));
      if (tb < 28) {
        if (lane < 32) {
          const f4 o = (G & 1) ? f4{cs[4], cs[5], cs[6], cs[7]} : f4{cs[0], cs[1], cs[2], cs[3]};
          st128_dev(rs, WS_CS + jt * 4096 + w * 1024 + mat * 512 + a16 * 32 + (G & 1) * 16, __builtin_bit_cast(u4, o));
        }
      }
      SB();
#pragma unroll
      for (int i = 0; i < 5; i++) kpiece(i);
      wait_vm<0>();
      if (tb < 28 && lane == 0) flag_store(ws, WS_FC + (jt * 8 + mat * 4 + w) * 8, tag);
#pragma unroll
      for (int i = 5; i < 9; i++) kpiece(i);
      WAIT_LGKM0();
      ST(3);
      BAR();
#pragma unroll
      for (int i = 9; i < 13; i++) kpiece(i);
      flag_wait<4>(ws, fwo, tag);
#pragma unroll
      for (int i = 0; i < 2 * NWR; i++) wr[i] = ld128_dev(rs, lane * 16, wso + i * 1024);
      flag_wait<4>(ws, WS_FX + lane * 8, tag);
      flag_wait<4>(ws, WS_FX + 512 + lane * 8, tag);
      gather_ld();
      gather_st();
      WAIT_LGKM0();
    }
    ST(7);
    BAR();
    BAR();
    ST(8);
    const u32 hr = sm0 + w * HREG;
    {
      const char* src = (const char*)wd + (size_t)(w * 28 + NWR) * 16384 + cb * 128;
      const u32 vo = (lane >> 3) * 1024 + (((lane & 7) ^ ((lane >> 2) & 4)) << 4);
#pragma unroll
      for (int i = 0; i < 2 * (28 - NWR); i++) dma16(hr + i * 1024, src + i * 8192, vo);
    }
    ST(9);
    flag_wait<1>(ws, WS_FH + (tb * 32 + w * 7 + (lane < 7 ? lane : 0)) * 8, tag);
    ST(10);
    f16v acc[4];
#pragma unroll
    for (int k = 0; k < 4; k++) acc[k] = f16v{};
    {
      constexpr int DEPTH = 6;
      const u32 hs = WS_H + (tb * 2 * 112 + w * 28) * 1024;
      auto ldH = [&](int i, int tk) -> u4 { return ld128_dev(rs, lane * 16, hs + (tk * 112 + i) * 1024); };
      const u32 tl = hr + (8 * (G >> 1) + qq) * 128 + 32 * (G & 1) + 8 * p;
      const u32 a0l = tl + 64 * (qq >> 1), a1l = tl + 64 * (1 - (qq >> 1));
      u4 B0[DEPTH], B1[DEPTH], an0, an1;
#pragma unroll
      for (int s = 0; s < DEPTH; s++) { B0[s] = ldH(s, 0); B1[s] = ldH(s, 1); }
#pragma unroll
      for (int s = 0; s < 28; s++) {
        const int r = s % DEPTH;
        const bf8 a0 = __builtin_bit_cast(bf8, s < NWR ? wr[2 * s] : an0), a1 = __builtin_bit_cast(bf8, s < NWR ? wr[2 * s + 1] : an1);
        const bf8 b0 = __builtin_bit_cast(bf8, B0[r]), b1 = __builtin_bit_cast(bf8, B1[r]);
        if (s + 1 >= NWR && s + 1 < 28) {
          const int o = (s + 1 - NWR) * 2048;
          an0 = cat(lds_tr(a0l + o), lds_tr(a0l + o + 512));
          an1 = cat(lds_tr(a1l + o), lds_tr(a1l + o + 512));
        }
        acc[0] = __builtin_amdgcn_mfma_f32_32x32x16_bf16(a0, b0, acc[0], 0, 0, 0);
        acc[2] = __builtin_amdgcn_mfma_f32_32x32x16_bf16(a1, b0, acc[2], 0, 0, 0);
        acc[1] = __builtin_amdgcn_mfma_f32_32x32x16_bf16(a0, b1, acc[1], 0, 0, 0);
        acc[3] = __builtin_amdgcn_mfma_f32_32x32x16_bf16(a1, b1, acc[3], 0, 0, 0);
        if (s + DEPTH < 28) { B0[r] = ldH(s + DEPTH, 0); B1[r] = ldH(s + DEPTH, 1); }
        SB();
      }
    }
    ST(11);
    f16v mine;
#pragma unroll
    for (int k = 0; k < 4; k++) {
      if (k == w) { mine = acc[k]; }
      else {
        const u32 a = hr + ((k - w - 1) & 3) * 4096 + lane * 16;
#pragma unroll
        for (int e = 0; e < 4; e++) lds_st<f4>(a + e * 1024, f4{acc[k][4 * e], acc[k][4 * e + 1], acc[k][4 * e + 2], acc[k][4 * e + 3]});
      }
    }
    WAIT_LGKM0();
    ST(12);
    BAR();
    ST(13);
    {
      const u32 l16 = sm0 + lane * 16;
      const u32 a0 = l16 + ((w + 3) & 3) * HREG, a1 = l16 + ((w + 2) & 3) * HREG + 4096, a2 = l16 + ((w + 1) & 3) * HREG + 8192;
      const u32 ab = scr(w ^ 1) + lane * 16;
      u2 o[4];
#pragma unroll
      for (int e = 0; e < 4; e++) {
        const f4 m4 = f4{mine[4 * e], mine[4 * e + 1], mine[4 * e + 2], mine[4 * e + 3]};
        const f4 v = ((lds_ld<f4>(a0 + e * 1024) + lds_ld<f4>(a1 + e * 1024)) + (lds_ld<f4>(a2 + e * 1024) + m4)) * r2 + lds_ld<f4>(ab + e * 1024);
        o[e] = u2{pk_bf16(v[0], v[1]), pk_bf16(v[2], v[3])};
      }
      u16* op = out + (size_t)(tb * 64 + (w & 1) * 32 + (lane & 31)) * D + cb * 64 + (w >> 1) * 32 + 8 * (lane >> 5);
#pragma unroll
      for (int e = 0; e < 4; e += 2) {
        auto r0 = __builtin_amdgcn_permlane32_swap(o[e][0], o[e + 1][0], false, false);
        auto r1 = __builtin_amdgcn_permlane32_swap(o[e][1], o[e + 1][1], false, false);
        *(u4*)(op + 8 * e) = u4{r0[0], r1[0], r0[1], r1[1]};
      }
    }
    ST(14);
  }
}

extern "C" void kernel_launch(void* const* d_in, const int* in_sizes, int n_in, void* d_out, int out_size,
                              void* d_ws, size_t ws_size, hipStream_t stream) {
  (void)hipFuncSetAttribute((const void*)fused, hipFuncAttributeMaxDynamicSharedMemorySize, LDS_BYTES);
  fused<<<256, 512, LDS_BYTES, stream>>>((const u16*)d_in[0], (const u16*)d_in[1], (const u16*)d_in[2], (const u16*)d_in[3],
                                         (const u16*)d_in[4], (const u16*)d_in[5], (const u16*)d_in[6], (const u16*)d_in[8],
                                         (const u16*)d_in[9], (const u16*)d_in[10], (char*)d_ws, (u16*)d_out);
}
```

```cpp
#include <hip/hip_runtime.h>
#include <hip/hip_bf16.h>

typedef unsigned short u16;
typedef unsigned int u32;
typedef unsigned long long u64;
typedef __attribute__((ext_vector_type(8))) __bf16 bf8;
typedef __attribute__((ext_vector_type(2))) __bf16 bf2;
typedef __attribute__((ext_vector_type(8))) _Float16 h8;
typedef __attribute__((ext_vector_type(2))) _Float16 h2;
typedef __attribute__((ext_vector_type(4))) u32 u4;
typedef __attribute__((ext_vector_type(2))) u32 u2;
typedef __attribute__((ext_vector_type(4))) short s4;
typedef __attribute__((ext_vector_type(16))) float f16v;
typedef __attribute__((ext_vector_type(4))) float f4;
typedef __attribute__((ext_vector_type(2))) float f2;
#define LAS __attribute__((address_space(3)))
#define GAS __attribute__((address_space(1)))
#define DEV __device__ __forceinline__

#define YSLEEP 16
#define HPRIO 2
#define HSLEEP 8
#define KROT 1

constexpr int D = 512, NM = 128;
constexpr u32 WS_H = 0;
constexpr u32 WS_WD = WS_H + 64 * 112 * 1024;
constexpr u32 WS_XT = WS_WD + 32 * 16 * 2048;
constexpr u32 WS_YT = WS_XT + 2 * 1024 * 1024;
constexpr u32 WS_SS = WS_YT + 4 * 1024 * 1024;
constexpr u32 WS_CS = WS_SS + 2048 * 8;
constexpr u32 FS = 256;
constexpr u32 WS_FX = WS_CS + 14 * 16384;
constexpr u32 WS_FA = WS_FX + 32 * FS;
constexpr u32 WS_FS = WS_FA + 32 * FS;
constexpr u32 WS_FW = WS_FS + 32 * FS;
constexpr u32 WS_FC = WS_FW + 32 * FS;
constexpr u32 WS_FH = WS_FC + 256 * FS;
constexpr u32 WS_EP = WS_FH + 2 * 32 * 32 * FS;
constexpr u32 WS_END = WS_EP + 256;

constexpr int KSZ = 11520;
constexpr int L_KS = 0;
constexpr int L_XR = 4 * KSZ;
constexpr int L_WR = L_XR + 49152;
constexpr int L_CS = L_WR + 65536;
constexpr int L_MS = L_CS + 1024;
constexpr int L_MI = L_MS + 512;
constexpr int LDS_BYTES = L_MI + 64;
constexpr int HREG = 26624;
constexpr int NWR = 15;
constexpr int L_UB = L_WR + 16384;
constexpr int NW_EARLY = 26;
constexpr int SPIN_CAP = 1 << 17;

DEV float blo(u32 v) { return __uint_as_float(v << 16); }
DEV float bhi(u32 v) { return __uint_as_float(v & 0xffff0000u); }
DEV u32 pk_bf16(float a, float b) { f2 v = {a, b}; return __builtin_bit_cast(u32, __builtin_convertvector(v, bf2)); }
DEV u32 pk_f16(float a, float b) { f2 v = {a, b}; return __builtin_bit_cast(u32, __builtin_convertvector(v, h2)); }
DEV u32 pk_f16_conj(f2 v) { u32 r; asm("v_cvt_pk_f16_f32 %0, %1, -%2" : "=v"(r) : "v"(v[0]), "v"(v[1])); return r; }
DEV bf2 as_bf2(u32 v) { return __builtin_bit_cast(bf2, v); }
DEV h2 as_h2(u32 v) { return __builtin_bit_cast(h2, v); }
DEV float rbf(float f) { return blo(pk_bf16(f, f)); }

DEV void dma16(u32 lds, const void* sbase, u32 voff) {
  asm volatile("s_mov_b32 m0, %0\n\ts_nop 0\n\tglobal_load_lds_dwordx4 %1, %2" ::"s"(lds), "v"(voff), "s"(sbase) : "memory");
}
DEV void set_m0(u32 lds) { asm volatile("s_mov_b32 m0, %0\n\ts_nop 0" ::"s"(lds) : "memory"); }
template <int OFF> DEV void dma_off(const void* sbase, u32 voff) {
  asm volatile("global_load_lds_dwordx4 %0, %1 offset:%2" ::"v"(voff), "s"(sbase), "n"(OFF) : "memory");
}
template <int OFF> DEV void lds_st_tid(u32 v) { asm volatile("ds_write_addtid_b32 %0 offset:%1" ::"v"(v), "n"(OFF) : "memory"); }
DEV u32 vm_pending() { u32 r; asm volatile("s_getreg_b32 %0, hwreg(HW_REG_IB_STS, 0, 4)" : "=s"(r)); return r; }
template <int N> DEV void wait_vm() { asm volatile("s_waitcnt vmcnt(%0)" ::"n"(N) : "memory"); }
#define WAIT_LGKM0() asm volatile("s_waitcnt lgkmcnt(0)" ::: "memory")
#define SB() __builtin_amdgcn_sched_barrier(0)
#define BAR() do { SB(); asm volatile("s_barrier" ::: "memory"); SB(); } while (0)

#define ST_DECL
#define ST(i)
#define TR(i)
#define ST_OUT()

template <int V> struct ic { static constexpr int value = V; };
template <int A, int B, class Fn> DEV void static_for(Fn&& f) {
  if constexpr (A < B) { f(ic<A>{}); static_for<A + 1, B>(f); }
}
template <class T> DEV T lds_ld(u32 a) { return *(LAS T*)(size_t)a; }
template <class T> DEV void lds_st(u32 a, T v) { *(LAS T*)(size_t)a = v; }
DEV u2 lds_tr(u32 a) { return __builtin_bit_cast(u2, __builtin_amdgcn_ds_read_tr16_b64_v4i16((LAS s4*)(size_t)a)); }
DEV u4 cat(u2 a, u2 b) { return u4{a[0], a[1], b[0], b[1]}; }

typedef __amdgpu_buffer_rsrc_t rsrc_t;
DEV u4 ld128_dev(rsrc_t r, u32 v, u32 s) { return __builtin_amdgcn_raw_buffer_load_b128(r, v, s, 16); }
DEV u2 ld64_dev(rsrc_t r, u32 v, u32 s) { return __builtin_amdgcn_raw_buffer_load_b64(r, v, s, 16); }
DEV void st128_dev(rsrc_t r, u32 v, u4 d) { __builtin_amdgcn_raw_buffer_store_b128(d, r, v, 0, 16); }
DEV void st64_dev(rsrc_t r, u32 v, u2 d) { __builtin_amdgcn_raw_buffer_store_b64(d, r, v, 0, 16); }
DEV void flag_store(char* ws, u32 off, u64 tag) {
  __hip_atomic_store((GAS u64*)(ws + off), tag, __ATOMIC_RELAXED, __HIP_MEMORY_SCOPE_AGENT);
}
DEV u64 flag_load(const char* ws, u32 off) {
  return __hip_atomic_load((GAS u64*)(ws + off), __ATOMIC_RELAXED, __HIP_MEMORY_SCOPE_AGENT);
}
template <int SLP> DEV void flag_wait(const char* ws, u32 off, u64 tag) {
  bool pend = true;
#pragma unroll 1
  for (int s = 0; s < SPIN_CAP; s++) {
    if (pend) pend = flag_load(ws, off) != tag;
    if (!__any(pend)) break;
    __builtin_amdgcn_s_sleep(SLP);
  }
  asm volatile("" ::: "memory");
}
DEV void flag_peek(u64& v, const char* ws, u32 off) {
  asm volatile("global_load_dwordx2 %0, %1, %2 sc1" : "=v"(v) : "v"(off), "s"(ws) : "memory");
}

DEV bool lds_arrive(u32 a, u32 n, int lane) {
  u32 old = 0;
  if (lane == 0) old = __hip_atomic_fetch_add((LAS u32*)(size_t)a, 1u, __ATOMIC_RELAXED, __HIP_MEMORY_SCOPE_WORKGROUP);
  return (u32)__builtin_amdgcn_readfirstlane(old) == n - 1;
}
DEV float xsum16(float v) { u32 u = __float_as_uint(v); auto r = __builtin_amdgcn_permlane16_swap(u, u, false, false); return __uint_as_float(r[0]) + __uint_as_float(r[1]); }
DEV float xsum32(float v) { u32 u = __float_as_uint(v); auto r = __builtin_amdgcn_permlane32_swap(u, u, false, false); return __uint_as_float(r[0]) + __uint_as_float(r[1]); }
DEV u32 xmax16(u32 u) { auto r = __builtin_amdgcn_permlane16_swap(u, u, false, false); return max(r[0], r[1]); }
DEV u32 xmax32(u32 u) { auto r = __builtin_amdgcn_permlane32_swap(u, u, false, false); return max(r[0], r[1]); }

template <bool FIRST> DEV void mma_bf16(f4& c, u4 a, u4 b) {
  if constexpr (FIRST) asm volatile("v_mfma_f32_16x16x32_bf16 %0, %1, %2, 0" : "=&v"(c) : "v"(a), "v"(b));
  else asm volatile("v_mfma_f32_16x16x32_bf16 %0, %1, %2, %0" : "+v"(c) : "v"(a), "v"(b));
}
template <bool FIRST> DEV void mma_f16(f4& c, u4 a, u4 b) {
  if constexpr (FIRST) asm volatile("v_mfma_f32_16x16x32_f16 %0, %1, %2, 0" : "=&v"(c) : "v"(a), "v"(b));
  else asm volatile("v_mfma_f32_16x16x32_f16 %0, %1, %2, %0" : "+v"(c) : "v"(a), "v"(b));
}
DEV void mfma_done() { SB(); asm volatile("s_nop 7"); SB(); }
DEV f2 cmul_pk(f2 a, f2 b) {
  f2 t;
  asm("v_pk_mul_f32 %0, %1, %2 op_sel_hi:[0,1]\n\tv_pk_fma_f32 %0, %1, %2, %0 op_sel:[1,1,0] op_sel_hi:[1,0,1] neg_lo:[0,1,0]"
      : "=&v"(t) : "v"(a), "v"(b));
  return t;
}
DEV void cmul_pk2(f2& o0, f2& o1, f2 a0, f2 b0, f2 a1, f2 b1) {
  asm("v_pk_mul_f32 %0, %2, %3 op_sel_hi:[0,1]\n\tv_pk_mul_f32 %1, %4, %5 op_sel_hi:[0,1]\n\t"
      "v_pk_fma_f32 %0, %2, %3, %0 op_sel:[1,1,0] op_sel_hi:[1,0,1] neg_lo:[0,1,0]\n\t"
      "v_pk_fma_f32 %1, %4, %5, %1 op_sel:[1,1,0] op_sel_hi:[1,0,1] neg_lo:[0,1,0]"
      : "=&v"(o0), "=&v"(o1) : "v"(a0), "v"(b0), "v"(a1), "v"(b1));
}
struct cf { float r, i; };
DEV cf cmul(cf a, cf b) {
  cf t;
  asm("v_mul_f32 %0, %2, %4\n\tv_mul_f32 %1, %2, %5\n\tv_fma_f32 %0, -%3, %5, %0\n\tv_fma_f32 %1, %3, %4, %1"
      : "=&v"(t.r), "=&v"(t.i) : "v"(a.r), "v"(a.i), "v"(b.r), "v"(b.i));
  return t;
}
DEV float mul1(float a, float b) { float t; asm("v_mul_f32 %0, %1, %2" : "=v"(t) : "v"(a), "v"(b)); return t; }
DEV float fma1(float a, float b, float c) { float t; asm("v_fma_f32 %0, %1, %2, %3" : "=v"(t) : "v"(a), "v"(b), "v"(c)); return t; }
DEV float add1(float a, float b) { float t; asm("v_add_f32 %0, %1, %2" : "=v"(t) : "v"(a), "v"(b)); return t; }
DEV cf cexp_at(float lr, float fr, float p) {
  float e = __builtin_amdgcn_exp2f(lr * p);
  float ph = __builtin_amdgcn_fractf(fr * p);
  float c = __builtin_amdgcn_cosf(ph), s = __builtin_amdgcn_sinf(ph);
  asm("s_nop 0" : "+v"(e), "+v"(c), "+v"(s));
  return cf{mul1(e, c), mul1(e, s)};
}
DEV u32 cidx_hi(int l) { return (pk_bf16((float)l, 0.f) & 0xffffu) - 17024u; }

struct grp { int t, n, by; };
constexpr grp SCHED[] = {
  {-1, 4, 0}, {-1, 4, 0}, {-1, 4, 1}, {-1, 4, 1}, {-1, 5, 2}, {-1, 4, 2},
  {0, 4, 3}, {0, 2, 3},
  {1, 2, 3}, {1, 4, 4}, {1, 2, 5},
  {2, 2, 5}, {2, 4, 5}, {2, 1, 5},
  {3, 2, 7}, {3, 4, 6},
  {4, 2, 7}, {4, 4, 7},
  {5, 2, 9}, {5, 4, 8}, {5, 1, 99},
  {7, 2, 9}, {7, 2, 11}, {7, 4, 9}, {7, 1, 10},
  {8, 4, 10}, {8, 4, 11},
  {9, 2, 11}, {9, 2, 12}, {9, 4, 12}, {9, 1, 12},
  {10, 2, 12}, {10, 4, 12}, {10, 2, 99},
  {11, 4, 13}, {11, 4, 14},
  {12, 6, 99},
  {13, 4, 99},
};
constexpr int n_wait(int bar) {
  int n = 0;
  for (const grp& g : SCHED) {
    if (g.t >= bar) break;
    n = g.by <= bar ? 0 : n + g.n;
  }
  return n;
}
__host__ __device__ constexpr int seq_t(int i) { return i < 96 ? i >> 3 : 12 + (((i - 96) >> 2) & 3); }
__host__ __device__ constexpr int seq_n(int i) { return i < 96 ? i & 7 : ((i - 96) & 3) + (i >= 112 ? 4 : 0); }
__host__ __device__ constexpr int w_slot(int t) {
  return t == 15 ? L_XR + 2 * 16384 : L_WR + (t <= 8 ? t & 3 : t == 9 || t == 13 ? 2 : t == 10 || t == 14 ? 3 : t == 11 ? 1 : 0) * 16384;
}
__host__ __device__ constexpr int ord2(int s) { return s < 14 ? (s >> 1) * 4 + (s & 1) : ((s - 14) >> 1) * 4 + 2 + (s & 1); }

__global__ __launch_bounds__(512, 2) void fused(const u16* __restrict__ x, const u16* __restrict__ lar,
                                                const u16* __restrict__ aim, const u16* __restrict__ cr,
                                                const u16* __restrict__ ci, const u16* __restrict__ ldt,
                                                const u16* __restrict__ dsk, const u16* __restrict__ wg,
                                                const u16* __restrict__ wu, const u16* __restrict__ wd,
                                                char* __restrict__ ws, u16* __restrict__ out) {
  extern __shared__ __attribute__((aligned(16))) char sm[];
  const u32 sm0 = (u32)(size_t)(LAS char*)sm;
  const int b = blockIdx.x, xcd = b & 7, qi = b >> 6, qj = (b >> 3) & 7;
  const bool isS = qj == 7;
  const int ti = (xcd >> 1) * 4 + qi, tj = (xcd & 1) * 7 + qj;
  const int tb = xcd * 4 + qi, cb = qj;
  const int wv = __builtin_amdgcn_readfirstlane(threadIdx.x >> 6);
  const int w = wv & 3;
  const int lane = threadIdx.x & 63;
  const int G = lane >> 4, qq = (lane & 15) >> 2, p = lane & 3, a16 = lane & 15;
  const int chl = w >> 1, hw = w & 1, ch = b * 2 + chl;
  const u32 ks = sm0 + L_KS + w * KSZ;
  const u32 xs = sm0 + L_KS + (2 * chl + 1) * KSZ, Pt = sm0 + L_KS + 2 * chl * KSZ;
  auto scr = [&](int v) -> u32 { return sm0 + ((v & 1) ? L_KS + v * KSZ + 4608 : L_XR + 4 * HREG + (v >> 1) * 4096); };
  const u32 ve = (lane >> 3) * 1024 + (((lane & 7) ^ (lane >> 4)) << 4);
  const u32 vd = (7 + (lane >> 3)) * 1024 + (((lane & 7) ^ (4 + (lane >> 4))) << 4);
  ST_DECL ST(0);

  if (wv < 4) {
    const rsrc_t rs = __builtin_amdgcn_make_buffer_rsrc(ws, 0, WS_END, 0x00020000);
    u32 p_lar, p_aim, p_ldt, p_dsk, p_cr, p_ci, p_cr2, p_ci2;
    u64 ep, tag;
    float kscale;
    auto ld_params = [&]() {
      const u32 o1 = (ch * NM + hw * 64 + lane) * 2, o2 = o1 ^ 128, o3 = ch * 2;
      asm volatile("global_load_ushort %0, %9, %13\n\tglobal_load_ushort %1, %9, %14\n\tglobal_load_ushort %2, %11, %17\n\t"
                   "global_load_ushort %4, %9, %15\n\tglobal_load_ushort %5, %9, %16\n\tglobal_load_ushort %6, %10, %15\n\t"
                   "global_load_ushort %7, %10, %16\n\tglobal_load_ushort %3, %11, %18\n\tglobal_load_dwordx2 %8, %12, %19 sc1"
                   : "=&v"(p_lar), "=&v"(p_aim), "=&v"(p_ldt), "=&v"(p_dsk), "=&v"(p_cr), "=&v"(p_ci), "=&v"(p_cr2), "=&v"(p_ci2), "=&v"(ep)
                   : "v"(o1), "v"(o2), "v"(o3), "v"(WS_EP), "s"(lar), "s"(aim), "s"(cr), "s"(ci), "s"(ldt), "s"(dsk), "s"(ws) : "memory");
    };
    auto wait_params = [&]() {
      asm volatile("s_waitcnt vmcnt(0)" : "+v"(p_lar), "+v"(p_aim), "+v"(p_ldt), "+v"(p_dsk), "+v"(p_cr), "+v"(p_ci), "+v"(p_cr2), "+v"(p_ci2), "+v"(ep) :: "memory");
    };
    u32 upk[21], vpk[32];
    auto gen_uv = [&]() {
      const float dt = rbf(__expf(blo(p_ldt)));
      const float lr = -rbf(__expf(blo(p_lar))) * dt * 1.44269504088896f;
      const float fr = blo(p_aim) * dt * 0.15915494309189535f;
      u32 cm = max(max(p_cr & 0x7fffu, p_ci & 0x7fffu), max(p_cr2 & 0x7fffu, p_ci2 & 0x7fffu));
      cm = max(cm, (u32)__builtin_amdgcn_update_dpp(0, (int)cm, 0xB1, 0xf, 0xf, true));
      cm = max(cm, (u32)__builtin_amdgcn_update_dpp(0, (int)cm, 0x4E, 0xf, 0xf, true));
      cm = max(cm, (u32)__builtin_amdgcn_update_dpp(0, (int)cm, 0x141, 0xf, 0xf, true));
      cm = max(cm, (u32)__builtin_amdgcn_update_dpp(0, (int)cm, 0x140, 0xf, 0xf, true));
      cm = xmax32(xmax16(cm)) >> 7;
      const float inv = __uint_as_float((254u - cm) << 23);
      kscale = 2.f * dt * __uint_as_float(cm << 23);
      const cf c1 = cexp_at(lr, fr, 1.f), c64 = cexp_at(lr, fr, 64.f);
      const f2 e1 = f2{c1.r, c1.i};
      f2 v[32], u[21], e2, e4, e256, e512, e1024;
      v[8] = f2{c64.r, c64.i};
      u[0] = f2{blo(p_cr) * inv, blo(p_ci) * inv};
      cmul_pk2(e2, v[16], e1, e1, v[8], v[8]);
      cmul_pk2(u[1], v[24], u[0], e1, v[8], v[16]);
      cmul_pk2(e4, e256, e2, e2, v[16], v[16]);
      cmul_pk2(u[2], u[3], u[0], e2, u[1], e2);
      cmul_pk2(v[1], e512, e4, e4, e256, e256);
      cmul_pk2(u[4], u[5], u[0], e4, u[1], e4);
      cmul_pk2(v[2], e1024, v[1], v[1], e512, e512);
      cmul_pk2(u[6], u[7], u[2], e4, u[3], e4);
      cmul_pk2(v[3], v[4], v[1], v[2], v[2], v[2]);
      cmul_pk2(u[8], u[9], u[0], e256, u[2], e256);
      cmul_pk2(v[5], v[6], v[1], v[4], v[2], v[4]);
      cmul_pk2(u[10], u[11], u[4], e256, u[6], e256);
      cmul_pk2(v[7], u[16], v[3], v[4], u[0], e1024);
      cmul_pk2(u[12], u[13], u[0], e512, u[4], e512);
      cmul_pk2(u[14], u[15], u[8], e512, u[10], e512);
      cmul_pk2(u[17], u[18], u[8], e1024, u[12], e1024);
      cmul_pk2(u[19], u[20], u[14], e1024, u[16], e1024);
#pragma unroll
      for (int j = 1; j < 8; j++) cmul_pk2(v[8 + j], v[16 + j], v[j], v[8], v[j], v[16]);
#pragma unroll
      for (int j = 1; j < 7; j += 2) cmul_pk2(v[24 + j], v[25 + j], v[j], v[24], v[j + 1], v[24]);
      v[31] = cmul_pk(v[7], v[24]);
#pragma unroll
      for (int i = 0; i < 21; i++) upk[i] = pk_f16_conj(u[i]);
      vpk[0] = 0x00003c00u;
#pragma unroll
      for (int j = 1; j < 32; j++) vpk[j] = pk_f16(v[j][0], v[j][1]);
      SB();
    };
    auto put_row = [&](auto J) {
      constexpr int j = decltype(J)::value;
      if constexpr (j == 0) set_m0(sm0 + L_UB - 61440 + w * 4096);
      if constexpr (j == 15) set_m0(ks);
      if constexpr (j < 15) lds_st_tid<61440 + j * 272>(upk[j]);
      else if constexpr (j < 21) lds_st_tid<8704 + (j - 15) * 272>(upk[j]);
      else if constexpr (j < 53) lds_st_tid<(j - 21) * 272>(vpk[j - 21]);
    };
    u4 bf[2][8], tf[2][8];
    u32 ba, pa[8];
    auto ldB = [&](int r, int i) { return lds_ld<u4>(ba + 192 - 64 * r + 512 * i); };
    auto conv_addr = [&]() {
      ba = xs + 2 * (240 + 16 * a16 + 8 * G - 16) - 256 * hw - 192;
      asm("" : "+v"(ba));
#pragma unroll
      for (int k = 0; k < 8; k++) { pa[k] = Pt + 4 * (2031 - a16 + 8 * G) - 512 * hw - 384 - 1024 * k; asm("" : "+v"(pa[k])); }
      SB();
    };
    auto conv_read = [&](int r, int m) {
      if (m >= 2 && m < 10) bf[r & 1][m - 2] = ldB(r, m - 2);
      else {
        const int kk = m < 2 ? 0 : (m - 8) >> 1, e = (m & 1) * 2;
        const u32 z = pa[kk] + 384 - 128 * r + e * 8;
        tf[r & 1][kk][e] = lds_ld<u32>(z); tf[r & 1][kk][e + 1] = lds_ld<u32>(z + 8);
      }
      SB();
    };
    u32 fho = WS_FH + ((ti * 2 + (w >> 1)) * 32 + tj * 2 + (w & 1)) * FS;
    int thr = -1;
    if (!isS) {
      ld_params();
      if (lane == 0) {
        lds_st<u32>(sm0 + L_MI + 32 + w * 4, 0u);
        lds_st<u32>(sm0 + L_MI + 48 + w * 4, 0u);
      }
      wait_params();
      ST(1);
      tag = ep + 1;
      gen_uv();

      ST(2);
      TR(1);
      BAR();
      ST(3);
      const int th = w >> 1, fh = w & 1;
      f4 acc[8][4];
      u32 ta[4];
#pragma unroll
      for (int cc = 0; cc < 4; cc++) ta[cc] = sm0 + L_WR + (8 * G + qq) * 256 + (((4 * fh + cc) ^ (qq | ((G & 1) << 2))) << 5) + 8 * p;
      const u32 xl = sm0 + L_XR + (th * 64 + a16) * 128;
      const u32 xa[2] = {xl + ((G ^ (a16 >> 1)) << 4), xl + (((4 + G) ^ (a16 >> 1)) << 4)};
      auto wadr = [&](int i) -> u32 { return ta[seq_n(i) >> 1] + (w_slot(seq_t(i)) - L_WR) + (seq_n(i) & 1) * 8192; };
      auto xadr = [&](int t, int j) -> u32 { return xa[t & 1] + ((t >> 1) % 3) * 16384 + j * 2048; };
      const u32 hs = WS_H + ((ti * 4 + th * 2) * 112 + tj * 8 + fh * 4) * 1024 + lane * 16;
      u4 hq[4];
      auto hstore = [&](int c0, int k) { st128_dev(rs, hs + ((k & 1) * 112 + c0 + (k >> 1)) * 1024, hq[k]); };
      float nm[4];
      f4 cg[2], cu[2];
      auto epi_load = [&](int c0) {
        if (c0 == 0) {
#pragma unroll
          for (int j = 0; j < 4; j++) nm[j] = -lds_ld<float>(sm0 + L_MS + (th * 64 + j * 16 + a16) * 4);
        }
#pragma unroll
        for (int z = 0; z < 2; z++) {
          cg[z] = lds_ld<f4>(sm0 + L_CS + (fh * 64 + (c0 + z) * 16 + 4 * G) * 4);
          cu[z] = lds_ld<f4>(sm0 + L_CS + 512 + (fh * 64 + (c0 + z) * 16 + 4 * G) * 4);
        }
        SB();
      };
      auto epilogue = [&](int c0) {
#pragma unroll
        for (int cc = c0; cc < c0 + 2; cc++) {
#pragma unroll
          for (int pp = 0; pp < 2; pp++) {
            u2 hv[2];
#pragma unroll
            for (int z = 0; z < 2; z++) {
              const int j = 2 * pp + z;
              const f4 pr = (acc[2 * cc][j] + cg[cc - c0] * nm[j]) * (acc[2 * cc + 1][j] + cu[cc - c0] * nm[j]);
              hv[z] = u2{pk_bf16(pr[0], pr[1]), pk_bf16(pr[2], pr[3])};
              if (c0 == 2) {
                SB();
#pragma unroll
                for (int m = 0; m < 3; m++) conv_read(0, 3 * (4 * (cc - c0) + 2 * pp + z) + m);
              }
            }
            auto r0 = __builtin_amdgcn_permlane16_swap(hv[0][0], hv[1][0], false, false);
            auto r1 = __builtin_amdgcn_permlane16_swap(hv[0][1], hv[1][1], false, false);
            hq[2 * (cc - c0) + pp] = u4{r0[0], r1[0], r0[1], r1[1]};
            if (c0 == 2 || (cc == c0 && pp == 0)) hstore(c0, 2 * (cc - c0) + pp);
          }
        }
      };
      u4 wq[4], xf[4], yf[4];
#pragma unroll
      for (int j = 0; j < 4; j++) xf[j] = lds_ld<u4>(xadr(0, j));
#pragma unroll
      for (int i = 0; i < 3; i++) wq[i] = cat(lds_tr(wadr(i)), lds_tr(wadr(i) + 1024));
      static_for<0, 128>([&](auto I) {
        constexpr int i = decltype(I)::value, t = seq_t(i), n = seq_n(i);
        constexpr bool full = i < 96;
        constexpr int f1 = full ? (n < 5 ? i + 3 : n == 6 ? i + 2 : n == 7 ? i + 3 : -1) : (i + 3 < 128 ? i + 3 : -1);
        constexpr int f2i = full && n == 6 ? i + 3 : -1;
        constexpr int pos = full ? n : (n & 3);
        constexpr int tn = full ? t + 1 : (i < 124 ? seq_t(i - pos + 4) : -1);
        if constexpr (i == 32) ST(4);
        if constexpr (i == 64) ST(5);
        if constexpr (i == 96) ST(6);
        if constexpr (i == 100 || i == 104) { TR(i == 100 ? 14 : 15); BAR(); }
        const u4 a = wq[i & 3];
        constexpr int q = i - 49, r0 = 6 * q, ra = 1, rb = 2, rc = 1, rd = 2;
        auto rows = [&](auto A, auto Nn) { if constexpr (q >= 0 && q < 9) static_for<decltype(A)::value, decltype(A)::value + decltype(Nn)::value>(put_row); };
        u2 lo, hi, lo2, hi2;
        mma_bf16<(i < 8)>(acc[n][0], a, xf[0]);
        SB();
        if constexpr (f1 >= 0) lo = lds_tr(wadr(f1));
        rows(ic<r0>{}, ic<ra>{});
        SB();
        mma_bf16<(i < 8)>(acc[n][1], a, xf[1]);
        SB();
        if constexpr (f1 >= 0) hi = lds_tr(wadr(f1) + 1024);
        rows(ic<r0 + ra>{}, ic<rb>{});
        SB();
        mma_bf16<(i < 8)>(acc[n][2], a, xf[2]);
        SB();
        if constexpr (full && pos < 4) yf[pos] = lds_ld<u4>(xadr(tn, pos));
        if constexpr (!full && pos < 2 && tn >= 0) yf[2 * pos] = lds_ld<u4>(xadr(tn, 2 * pos));
        if constexpr (f2i >= 0) lo2 = lds_tr(wadr(f2i));
        rows(ic<r0 + ra + rb>{}, ic<rc>{});
        SB();
        mma_bf16<(i < 8)>(acc[n][3], a, xf[3]);
        SB();
        if constexpr (!full && pos < 2 && tn >= 0) yf[2 * pos + 1] = lds_ld<u4>(xadr(tn, 2 * pos + 1));
        if constexpr (f2i >= 0) hi2 = lds_tr(wadr(f2i) + 1024);
        rows(ic<r0 + ra + rb + rc>{}, ic<rd>{});
        if constexpr (f1 >= 0) wq[f1 & 3] = cat(lo, hi);
        if constexpr (f2i >= 0) wq[f2i & 3] = cat(lo2, hi2);
        SB();
        if constexpr (full && n == 5) {
          WAIT_LGKM0();
          TR(t + 2);
          BAR();
        }
        if constexpr ((full && n == 7) || (!full && pos == 3 && tn >= 0)) {
#pragma unroll
          for (int j = 0; j < 4; j++) xf[j] = yf[j];
        }
        if constexpr (i == 111) { ST(16); TR(16); BAR(); epi_load(0); mfma_done(); epilogue(0); SB(); ST(17); }
        if constexpr (i >= 112 && i < 115) { hstore(0, i - 111); SB(); }
        if constexpr (i == 125) epi_load(2);
        if constexpr (i == 126) conv_addr();
      });
      ST(7);
      TR(17);
      BAR();
      thr = 4;
      if (vm_pending() == 0) {
        wait_vm<0>();
        if (lane == 0) flag_store(ws, fho, tag);
        fho += 1024 * FS;
        thr = 0;
      }
      mfma_done();
      epilogue(2);
    } else {
      const u32 sreg = sm0 + L_WR + w * 16640;
      {
        const char* xb = (const char*)x + (size_t)(tb * 64 + w * 16) * 1024;
#pragma unroll
        for (int c = 0; c < 8; c++) { set_m0(sreg + c * 2048); dma_off<0>(xb + c * 128, ve); dma_off<1024>(xb + c * 128, vd); }
      }
      ld_params();
      if (lane == 0) { lds_st<u32>(sm0 + L_MI + 32 + w * 4, 0u); lds_st<u32>(sm0 + L_MI + 48 + w * 4, 0u); }
      WAIT_LGKM0();
      BAR();
      wait_params();
      ST(1);
      tag = ep + 1;
      u4 xr[16];
      const u32 xa0 = sreg + a16 * 128 + ((G ^ (a16 >> 1)) << 4), xa1 = sreg + a16 * 128 + (((4 + G) ^ (a16 >> 1)) << 4);
#pragma unroll
      for (int k = 0; k < 16; k++) xr[k] = lds_ld<u4>(((k & 1) ? xa1 : xa0) + (k >> 1) * 2048);
      float s1 = 0.f, s2 = 0.f;
      const bf2 ones = as_bf2(0x3f803f80u);
#pragma unroll
      for (int k = 0; k < 16; k++)
#pragma unroll
        for (int e = 0; e < 4; e++) {
          bf2 v = as_bf2(xr[k][e]);
          s1 = __builtin_amdgcn_fdot2_f32_bf16(v, ones, s1, false);
          s2 = __builtin_amdgcn_fdot2_f32_bf16(v, v, s2, false);
        }
      const float mean = xsum32(xsum16(s1)) * (1.f / D);
      const float var = fmaxf(xsum32(xsum16(s2)) * (1.f / D) - mean * mean, 0.f);
      const float sd = rbf(__builtin_amdgcn_sqrtf(rbf(rbf(var) + 1e-5f)));
      const float rsd = __builtin_amdgcn_rcpf(sd), mb = rbf(mean), rc = -mb * rsd;
      if (lane < 16) st64_dev(rs, WS_SS + (tb * 64 + w * 16 + lane) * 8, u2{__float_as_uint(mb), __float_as_uint(rsd * rsd)});
      WAIT_LGKM0();
      ST(2);
      const u32 ra = sreg + (8 * (G & 1) + qq) * 1040 + 32 * (G >> 1) + 8 * p;
      const u32 xo = WS_XT + ((4 * (G >> 1) + (a16 >> 2)) * 128 + tb * 4 + w) * 128 + (a16 & 3) * 32 + 16 * (G & 1);
#pragma unroll
      for (int k = 0; k < 17; k++) {
        if (k < 16) {
          u4 o;
#pragma unroll
          for (int e = 0; e < 4; e++) o[e] = pk_f16(__builtin_fmaf(blo(xr[k][e]), rsd, rc), __builtin_fmaf(bhi(xr[k][e]), rsd, rc));
          lds_st<u4>(sreg + a16 * 1040 + k * 64 + G * 16, o);
          xr[k] = cat(lds_tr(ra + k * 64), lds_tr(ra + k * 64 + 4160));
        }
        if (k > 0) st128_dev(rs, xo + (k - 1) * (8 * 128 * 128), xr[k - 1]);
        if (k == 6) {
          wait_vm<6>();
          if (lds_arrive(sm0 + L_MI + 56, 4, lane) && lane == 0) flag_store(ws, WS_FS + tb * FS, tag);
        }
        SB();
      }
      ST(3);
      gen_uv();
      wait_vm<0>();
      if (lds_arrive(sm0 + L_MI + 48, 4, lane) && lane == 0) flag_store(ws, WS_FX + tb * FS, tag);
      ST(6);
      BAR();
      static_for<0, 53>(put_row);
      WAIT_LGKM0();
      BAR();
      if (w == 0 && lane == 0) flag_store(ws, WS_FA + tb * FS, tag);
      BAR();
      if (tb == 0 && w == 0) {
#pragma unroll 1
        for (int k = 0; k < 4; k++) {
          const int i = k * 64 + lane;
          flag_wait<8>(ws, i < 224 ? WS_FC + i * FS : WS_FA + (i - 224) * FS, tag);
        }
        if (lane == 0) flag_store(ws, WS_EP, tag);
      }
      ST(7);
      BAR();
      conv_addr();
#pragma unroll
      for (int m = 0; m < 24; m++) conv_read(0, m);
    }
    const float v_dsk = blo(p_dsk);
    TR(18);
    ST(8);
    const int qt = w & 1, qc = w >> 1;
    u2 xv[4];
    f4 cac[8];
    {
      auto publish_now = [&]() {
        wait_vm<4>();
        if (thr == 1) wait_vm<1>();
        if (thr == 0) wait_vm<0>();
        if (lane == 0) flag_store(ws, fho, tag);
        fho += 1024 * FS;
        thr = thr == 4 ? 1 : -1;
      };
      auto publish = [&](bool force) {
        if (force) {
          if (thr == 4) publish_now();
          if (thr >= 0) publish_now();
        } else if (__builtin_expect((int)vm_pending() <= thr, 0)) {
          publish_now();
        }
        SB();
      };
      publish(false);
      static_for<0, 144>([&](auto I) {
        constexpr int i = decltype(I)::value, r = i / 36, n = i % 36;
        constexpr int k = n < 8 ? 0 : n < 15 ? 1 : n < 21 ? 2 : n < 26 ? 3 : n < 30 ? 4 : n < 33 ? 5 : n < 35 ? 6 : 7;
        constexpr int j = k + n - (k * (17 - k)) / 2;
        mma_f16<(i < 8)>(cac[j], tf[r & 1][k], bf[r & 1][j - k]);
        SB();
        if constexpr (r < 3 && n % 3 != 2) conv_read(r + 1, n - n / 3);
        if constexpr (n % 12 == 11) publish(i == 143);
      });
      mfma_done();
      const u16* xp = x + (size_t)(tb * 64 + qt * 32 + (lane & 31)) * D + cb * 64 + qc * 32 + 4 * (lane >> 5);
#pragma unroll
      for (int e = 0; e < 4; e++) xv[e] = *(const u2*)(xp + 8 * e);
    }
    ST(10);
    const u32 mine = scr(w), oth = scr(w ^ 1);
    const u32 t16 = (u32)tag & 0xffffu;
    const u32 ytag[4] = {t16, t16 ^ 0x5a33u, t16 ^ 0xc3a5u, t16 ^ 0x96ccu};
    {
      f4 keep[4];
#pragma unroll
      for (int j = 0; j < 4; j++) {
        f4 snd = hw ? cac[j] : cac[4 + j];
        keep[j] = hw ? cac[4 + j] : cac[j];
        lds_st<f4>(mine + j * 1024 + lane * 16, snd);
      }
      WAIT_LGKM0();
      if (lane == 0) *(volatile LAS u32*)(size_t)(sm0 + L_MI + 32 + w * 4) = 1u;
#pragma unroll 1
      for (int s = 0; s < SPIN_CAP; s++) {
        if (*(volatile LAS u32*)(size_t)(sm0 + L_MI + 32 + (w ^ 1) * 4) != 0u) break;
      }
      asm volatile("" ::: "memory");
      ST(19);
      const u32 yo = WS_YT + ((32 * hw + (a16 >> 1)) * 512 + ch) * 128 + (a16 & 1) * 64 + G * 16;
#pragma unroll
      for (int j = 0; j < 4; j++) {
        const f4 v = lds_ld<f4>(oth + j * 1024 + lane * 16);
        u2 xn2 = lds_ld<u2>(xs + 480 + 2 * (1024 * hw + 256 * j + 16 * a16 + 4 * G));
        h2 x01 = as_h2(xn2[0]), x23 = as_h2(xn2[1]);
        float r[4];
        const float xe[4] = {(float)x01[0], (float)x01[1], (float)x23[0], (float)x23[1]};
#pragma unroll
        for (int e = 0; e < 4; e++) r[e] = fma1(add1(keep[j][e], v[e]), kscale, mul1(v_dsk, xe[e]));
        const u32 p0 = pk_bf16(r[0], r[1]), p1 = pk_bf16(r[2], r[3]);
        st128_dev(rs, yo + j * (8 * 512 * 128), u4{(p0 << 16) | ytag[0], (p0 & 0xffff0000u) | ytag[1], (p1 << 16) | ytag[2], (p1 & 0xffff0000u) | ytag[3]});
      }
    }
    ST(11);
    {
      const u32 ys = WS_YT + ((tb * 2 + qt) * 512 + cb * 64 + qc * 32) * 128;
      u4 yg[4];
      bool pend[4] = {true, true, true, true};
      __builtin_amdgcn_s_sleep(YSLEEP);
#pragma unroll 1
      for (int s = 0; s < SPIN_CAP; s++) {
#pragma unroll
        for (int i = 0; i < 4; i++)
          if (pend[i]) yg[i] = ld128_dev(rs, lane * 16, ys + i * 1024);
        asm volatile("" ::: "memory");
#pragma unroll
        for (int i = 0; i < 4; i++)
          pend[i] = ((((yg[i][0] ^ ytag[0]) | (yg[i][1] ^ ytag[1])) | ((yg[i][2] ^ ytag[2]) | (yg[i][3] ^ ytag[3]))) & 0xffffu) != 0u;
        if (!__any(pend[0] || pend[1] || pend[2] || pend[3])) break;
        __builtin_amdgcn_s_sleep(2);
      }
      ST(12);
#pragma unroll
      for (int i = 0; i < 4; i++)
        lds_st<u2>(oth + i * 512 + lane * 8, u2{__builtin_amdgcn_perm(yg[i][1], yg[i][0], 0x07060302u), __builtin_amdgcn_perm(yg[i][3], yg[i][2], 0x07060302u)});
      const u32 ya = oth + (4 * (G >> 1) + qq) * 64 + 32 * (G & 1) + 8 * p;
      u2 yv[4];
#pragma unroll
      for (int e = 0; e < 4; e++) yv[e] = lds_tr(ya + e * 512);
      WAIT_LGKM0();
#pragma unroll
      for (int e = 0; e < 4; e++)
        lds_st<f4>(oth + e * 1024 + lane * 16,
                   f4{add1(blo(xv[e][0]), blo(yv[e][0])), add1(bhi(xv[e][0]), bhi(yv[e][0])), add1(blo(xv[e][1]), blo(yv[e][1])), add1(bhi(xv[e][1]), bhi(yv[e][1]))});
    }
    WAIT_LGKM0();
    ST(13);
    TR(19);
    BAR();
  } else {
    const rsrc_t rs = __builtin_amdgcn_make_buffer_rsrc(ws, 0, WS_END, 0x00020000);
    u4 vb[8], ua[8], cw[4], cso;
    f16v cs;
    f16v dk;
    const int r32 = lane & 31;
    const u32 fra = ks + r32 * 272 + (lane >> 5) * 16;
    const u32 fru = (r32 < 15 ? sm0 + L_UB + w * 4096 + r32 * 272 : ks + 8704 + (min(r32, 20) - 15) * 272) + (lane >> 5) * 16;
    auto kop = [&](auto I) {
      constexpr int i = decltype(I)::value;
      if constexpr (i == 30) {
        u4 sel;
#pragma unroll
        for (int e = 0; e < 4; e++) sel[e] = r32 == 2 * e ? 0x00003f80u : r32 == 2 * e + 1 ? 0x3f800000u : 0u;
        cs = f16v{};
        SB();
        __builtin_amdgcn_s_setprio(3);
#pragma unroll
        for (int ii = 0; ii < 4; ii++) cs = __builtin_amdgcn_mfma_f32_32x32x16_bf16(__builtin_bit_cast(bf8, sel), __builtin_bit_cast(bf8, cw[ii]), cs, 0, 0, 0);
        __builtin_amdgcn_s_setprio(0);
      } else if constexpr (i > 30) {
        const u32 c0 = __float_as_uint(cs[i - 31]);
        auto r = __builtin_amdgcn_permlane16_swap(c0, c0, false, false);
        float v = add1(__uint_as_float(r[0]), __uint_as_float(r[1]));
        asm("s_nop 1" : "+v"(v));
        cso[i - 31] = __float_as_uint(add1(v, __int_as_float(__builtin_amdgcn_update_dpp(0, __float_as_int(v), 0x128, 0xf, 0xf, false))));
      } else if constexpr (i < 8) ua[i] = lds_ld<u4>(fru + i * 32);
      else if constexpr (i < 16) vb[i - 8] = lds_ld<u4>(fra + (i - 8) * 32);
      else {
        if constexpr (i == 16) dk = f16v{};
        __builtin_amdgcn_s_setprio(3);
        dk = __builtin_amdgcn_mfma_f32_32x32x16_f16(__builtin_bit_cast(h8, ua[i - 16]), __builtin_bit_cast(h8, vb[i - 16]), dk, 0, 0, 0);
        __builtin_amdgcn_s_setprio(0);
      }
      SB();
    };
    auto put_ct = [&]() {
      const u32 ct = ks + 8704;
      const int h = lane >> 5;
      lds_st<f4>(ct + (8 * r32 + 4 * h) * 4, f4{dk[0], dk[1], dk[2], dk[3]});
      const int i1 = h ? (384 + 2 * r32) : (256 + 4 * r32), i2 = h ? (448 + 2 * r32) : (258 + 4 * r32);
      lds_st<f2>(ct + i1 * 4, f2{dk[4], dk[5]});
      lds_st<f2>(ct + i2 * 4, f2{dk[6], dk[7]});
      lds_st<float>(ct + (512 + 128 * h + r32) * 4, dk[8]);
      lds_st<float>(ct + (544 + 128 * h + r32) * 4, dk[9]);
      if (h == 0) { lds_st<float>(ct + (576 + r32) * 4, dk[10]); lds_st<float>(ct + (608 + r32) * 4, dk[11]); }
    };
    auto expand = [&](auto&& between) {
      float ev[32];
      u32 ea;
#pragma unroll
      for (int z = 0; z < 16; z++) {
        if ((z & 3) == 0) {
          ea = cidx_hi(1024 * hw + 64 * z + lane);
          if (z == 0) ea = hw ? ea : (u32)lane;
          ea = sm0 + L_KS + 2 * chl * KSZ + 8704 + ea * 4;
        } else {
          ea += hw ? 32u : (z < 4 ? 256u : z < 8 ? 128u : 64u);
        }
        ev[2 * z] = lds_ld<float>(ea); ev[2 * z + 1] = lds_ld<float>(ea + KSZ);
      }
      SB();
      const u32 pw = Pt + (2047 - 1024 * hw - 960 - lane) * 4;
      float kprev;
      u32 pk[16];
      static_for<0, 16>([&](auto Z) {
        constexpr int z = decltype(Z)::value;
        between(z);
        float k = add1(ev[2 * z], ev[2 * z + 1]);
        asm("s_nop 0" : "+v"(k));
        float old;
        if constexpr (z == 0) old = hw ? k : 0.f;
        else old = __int_as_float(__builtin_amdgcn_update_dpp(0, __float_as_int(kprev), 0x13C, 0xf, 0xf, false));
        const float km = __int_as_float(__builtin_amdgcn_update_dpp(__float_as_int(old), __float_as_int(k), 0x138, 0xf, 0xf, false));
        kprev = k;
        pk[z] = pk_f16(k, km);
        SB();
      });
      __builtin_amdgcn_s_setprio(3);
#pragma unroll
      for (int z = 0; z < 16; z++) lds_st<u32>(pw + (15 - z) * 256, pk[z]);
      if (!hw && lane < 34) lds_st<u4>(lane < 30 ? xs + lane * 16 : Pt + 8192 + (lane - 30) * 16, u4{0, 0, 0, 0});
    };
    u4 wr[2 * NWR];
    const u32 wso = WS_WD + (cb * 4 + w) * 32 * 1024;
    const u32 fwo = WS_FW + (cb * 4 + w) * FS;
    auto ldW = [&](int i) { wr[i] = ld128_dev(rs, lane * 16, wso + i * 1024); };
    u64 tag;
    float r2;
    u4 gx0, gx1;
    auto gather_ld = [&]() {
      const u32 so = WS_XT + ((ch >> 2) * 128 + hw * 64) * 128 + (ch & 3) * 32, vo = (lane >> 1) * 128 + (lane & 1) * 16;
      gx0 = ld128_dev(rs, vo, so); gx1 = ld128_dev(rs, vo, so + 4096);
      r2 = __uint_as_float(__builtin_amdgcn_raw_buffer_load_b32(rs, ((w & 1) * 32 + (lane & 31)) * 8 + 4, WS_SS + tb * 512, 16));
    };
    auto gather_st = [&]() {
      lds_st<u4>(xs + 480 + hw * 2048 + lane * 16, gx0);
      lds_st<u4>(xs + 480 + hw * 2048 + 1024 + lane * 16, gx1);
    };
    if (!isS) {
      const int rot = KROT * 2 * (xcd >> 1);
      const char* xb = (const char*)x + (size_t)(ti * 128 + w * 32) * 1024;
      const char* wb = (const char*)((w & 2) ? wu : wg) + (size_t)(16 * (w & 1)) * 3584 + tj * 256;
      u32 wvo[4];
#pragma unroll
      for (int ii = 0; ii < 4; ii++) wvo[ii] = (4 * ii + G) * 3584 + ((a16 ^ (2 * (G | ((ii >> 1) << 2)))) << 4) - ii * 1024;
      auto fill = [&](auto K, auto E) { constexpr int k = decltype(K)::value; if constexpr (k >= 0 && k < decltype(E)::value) kop(ic<k>{}); };
      auto dmaX = [&](auto S, auto HALF, auto K, auto E) {
        constexpr int s = decltype(S)::value, half = decltype(HALF)::value, k = decltype(K)::value;
        const char* src = xb + ((s + rot) & 7) * 128 + half * 16384;
        set_m0(sm0 + L_XR + (s % 3) * 16384 + w * 4096 + half * 2048);
        dma_off<0>(src, ve); fill(ic<k>{}, E);
        dma_off<1024>(src, vd); fill(ic<(k < 0 ? -1 : k + 1)>{}, E);
      };
      auto dmaW = [&](auto T, auto K, auto E) {
        constexpr int t = decltype(T)::value, k = decltype(K)::value;
        const char* src = wb + (size_t)((t + 2 * rot) & 15) * (32 * 3584);
        set_m0(sm0 + w_slot(t) + w * 4096);
        dma_off<0>(src, wvo[0]); fill(ic<k>{}, E);
        dma_off<1024>(src, wvo[1]); fill(ic<(k < 0 ? -1 : k + 1)>{}, E);
        dma_off<2048>(src, wvo[2]); fill(ic<(k < 0 ? -1 : k + 2)>{}, E);
        dma_off<3072>(src, wvo[3]); fill(ic<(k < 0 ? -1 : k + 3)>{}, E);
      };
      using N = ic<-1>;
      dmaX(ic<0>{}, ic<0>{}, N{}, N{}); dmaX(ic<0>{}, ic<1>{}, N{}, N{}); dmaW(ic<0>{}, N{}, N{});
      dmaW(ic<1>{}, N{}, N{}); dmaX(ic<1>{}, ic<0>{}, N{}, N{}); dmaX(ic<1>{}, ic<1>{}, N{}, N{});
      u64 ep;
      {
        const char* src = (const char*)((w & 2) ? wu : wg) + (size_t)(32 * ti + (lane >> 3)) * 3584 + tj * 256 + (w & 1) * 128 + (lane & 7) * 16;
#pragma unroll
        for (int ii = 0; ii < 4; ii++)
          asm volatile("global_load_dwordx4 %0, %1, off" : "=v"(cw[ii]) : "v"(src + ii * 8 * 3584) : "memory");
        flag_peek(ep, ws, WS_EP);
      }
      dmaW(ic<2>{}, N{}, N{});
      ST(1);
      wait_vm<n_wait(0)>();
      ST(2);
      TR(1);
      BAR();
      u64 f0, f1, f2_;
      u4 cpart[4];
      u32 mval;
      const u32 fco = lane == 16 ? WS_FS + (2 * ti + (w >> 1)) * FS : WS_FC + (tj * 16 + a16) * FS;
      const u32 fxo = lane == 16 ? WS_FX + tb * FS : WS_FX + (16 * hw + a16) * FS;
      static_for<0, 15>([&](auto T) {
        constexpr int t = decltype(T)::value;
        if constexpr (t == 0) { dmaW(ic<3>{}, N{}, N{}); dmaX(ic<2>{}, ic<0>{}, N{}, N{}); }
        if constexpr (t == 1) { dmaX(ic<2>{}, ic<1>{}, N{}, N{}); dmaW(ic<4>{}, N{}, N{}); dmaX(ic<3>{}, ic<0>{}, N{}, N{}); }
        if constexpr (t == 2) {
          asm volatile("" : "+v"(cw[0]), "+v"(cw[1]), "+v"(cw[2]), "+v"(cw[3]), "+v"(ep) :: "memory");
          tag = ep + 1;
          kop(ic<30>{});
          dmaX(ic<3>{}, ic<1>{}, N{}, N{}); dmaW(ic<5>{}, ic<31>{}, ic<35>{});
          if (r32 < 8) st128_dev(rs, WS_CS + (tj * 16 + ti) * 1024 + w * 256 + r32 * 32 + (lane >> 5) * 16, cso);
        }
        if constexpr (t >= 3 && t <= 5) { dmaX(ic<((t + 5) >> 1)>{}, ic<((t + 5) & 1)>{}, N{}, N{}); dmaW(ic<t + 3>{}, N{}, N{}); }
        if constexpr (t == 5) { if (lane == 0) flag_store(ws, WS_FC + (tj * 16 + ti) * FS, tag); }
        if constexpr (t == 7) { dmaX(ic<5>{}, ic<1>{}, N{}, N{}); dmaX(ic<6>{}, ic<0>{}, N{}, N{}); dmaW(ic<9>{}, N{}, N{}); flag_peek(f0, ws, fwo); }
        if constexpr (t == 8) {
          __builtin_amdgcn_s_setprio(3);
          static_for<0, 8>(kop);
          __builtin_amdgcn_s_setprio(0);
          dmaW(ic<10>{}, N{}, N{});
          WAIT_LGKM0();
          dmaW(ic<11>{}, N{}, N{});
        }
        if constexpr (t == 9) {
          dmaX(ic<6>{}, ic<1>{}, ic<8>{}, ic<16>{}); dmaX(ic<7>{}, ic<0>{}, ic<10>{}, ic<16>{}); dmaW(ic<12>{}, ic<12>{}, ic<16>{});
          flag_peek(f1, ws, fco);
        }
        if constexpr (t == 10) {
          dmaX(ic<7>{}, ic<1>{}, N{}, N{}); dmaW(ic<13>{}, ic<16>{}, ic<20>{});
          asm volatile("" : "+v"(f0) :: "memory");
          if (__builtin_expect(!__all(f0 == tag), 0)) flag_wait<1>(ws, fwo, tag);
          ldW(0); ldW(1);
        }
        if constexpr (t == 11) { dmaW(ic<14>{}, ic<20>{}, ic<24>{}); dmaW(ic<15>{}, N{}, N{}); }
        if constexpr (t == 12) {
          asm volatile("" : "+v"(f1) :: "memory");
          if (__builtin_expect(!__all(f1 == tag), 0)) flag_wait<1>(ws, fco, tag);
#pragma unroll
          for (int i = 0; i < 4; i++) cpart[i] = ld128_dev(rs, G * 1024 + a16 * 16, WS_CS + (tj * 16 + 4 * i) * 1024 + w * 256);
          mval = __builtin_amdgcn_raw_buffer_load_b32(rs, (lane & 31) * 8, WS_SS + (ti * 128 + w * 32) * 8, 16);
          f2_ = flag_load(ws, fxo);
        }
        if constexpr (t == 13) {
#pragma unroll
          for (int i = 2; i < 6; i++) ldW(i);
        }
        if constexpr (t == 14) {
#pragma unroll
          for (int i = 6; i < 10; i++) ldW(i);
          SB();
          __builtin_amdgcn_s_setprio(3);
          put_ct();
          f4 c4;
#pragma unroll
          for (int e = 0; e < 4; e++) {
            const u32 s4_ = __float_as_uint(add1(add1(__uint_as_float(cpart[0][e]), __uint_as_float(cpart[1][e])),
                                                 add1(__uint_as_float(cpart[2][e]), __uint_as_float(cpart[3][e]))));
            auto r = __builtin_amdgcn_permlane16_swap(s4_, s4_, false, false);
            const u32 v = __float_as_uint(add1(__uint_as_float(r[0]), __uint_as_float(r[1])));
            auto r2_ = __builtin_amdgcn_permlane32_swap(v, v, false, false);
            c4[e] = add1(__uint_as_float(r2_[0]), __uint_as_float(r2_[1]));
          }
          lds_st<f4>(sm0 + L_CS + w * 256 + a16 * 16, c4);
          lds_st<u32>(sm0 + L_MS + (w * 32 + (lane & 31)) * 4, mval);
          __builtin_amdgcn_s_setprio(0);
        }
        if constexpr (t == 11) ST(3);
        if constexpr (t == 12) ST(4);
        if constexpr (t == 13) ST(5);
        if constexpr (t == 14) ST(6);
        if constexpr (t <= 13) wait_vm<n_wait(t + 1)>();
        WAIT_LGKM0();
        if constexpr (t == 11) ST(16);
        if constexpr (t == 12) ST(17);
        if constexpr (t == 13) ST(18);
        TR(t + 2);
        BAR();
      });
      if (__builtin_expect(!__all(f2_ == tag), 0)) flag_wait<1>(ws, fxo, tag);
      asm volatile("" ::: "memory");
      gather_ld();
      expand([&](int z) { ldW(10 + z); SB(); });
      gather_st();
      __builtin_amdgcn_s_setprio(0);
      ST(21);
      WAIT_LGKM0();
    } else {
      u64 ep;
      flag_peek(ep, ws, WS_EP);
      const u32 bo = sm0 + L_XR + w * 8192;
      {
        const char* src = (const char*)wd + (size_t)((tb & 3) * 28) * 16384 + (tb >> 2) * 128;
        const u32 vo = (lane >> 3) * 1024 + (lane & 7) * 16;
#pragma unroll
        for (int i = 0; i < 4; i++) {
          const int kk = 4 * w + i, kst = kk < 14 ? (kk >> 1) * 4 + (kk & 1) : kk - 12;
          if (kk < NWR) {
            set_m0(bo + i * 2048);
            dma_off<0>(src + kst * 16384, vo);
            dma_off<1024>(src + kst * 16384, vo + 7168);
          }
        }
      }
      BAR();
      asm volatile("s_waitcnt vmcnt(0)" : "+v"(ep) :: "memory");
      tag = ep + 1;
      ST(1);
      {
        const u32 ra = bo + (8 * (G >> 1) + qq) * 128 + 32 * (G & 1) + 8 * p;
#pragma unroll
        for (int i = 0; i < 4; i++)
#pragma unroll
          for (int e = 0; e < 2; e++)
            if (4 * w + i < NWR) st128_dev(rs, WS_WD + ((tb * 16 + 4 * w + i) * 2 + e) * 1024 + lane * 16,
                      cat(lds_tr(ra + i * 2048 + e * 64), lds_tr(ra + i * 2048 + e * 64 + 512)));
      }
      wait_vm<0>();
      if (lds_arrive(sm0 + L_MI + 52, 4, lane) && lane == 0) flag_store(ws, WS_FW + tb * FS, tag);
      WAIT_LGKM0();
      ST(3);
      BAR();
      BAR();
      static_for<0, 24>(kop);
      put_ct();
      WAIT_LGKM0();
      BAR();
      expand([](int) {});
      __builtin_amdgcn_s_setprio(0);
      flag_wait<4>(ws, fwo, tag);
#pragma unroll
      for (int i = 0; i < NW_EARLY; i++) ldW(i);
      flag_wait<4>(ws, WS_FX + (lane & 31) * FS, tag);
      gather_ld();
      gather_st();
      WAIT_LGKM0();
    }
    ST(7);
    TR(17);
    BAR();
    if (!isS) __builtin_amdgcn_s_sleep(HSLEEP);
    ST(8);
#pragma unroll
    for (int i = NW_EARLY; i < 2 * NWR; i++) ldW(i);
    const u32 hr = sm0 + L_XR + w * HREG;
    {
      const char* src = (const char*)wd + (size_t)(w * 28) * 16384 + cb * 128;
      const u32 vo = (lane >> 3) * 1024 + (((lane & 7) ^ ((lane >> 2) & 4)) << 4);
#pragma unroll
      for (int i = NWR; i < 28; i++) {
        set_m0(hr + (i - NWR) * 2048);
        dma_off<0>(src + ord2(i) * 16384, vo);
        dma_off<1024>(src + ord2(i) * 16384, vo + 7168);
      }
    }
    ST(9);
    const u32 fho = WS_FH + (tb * 32 + w * 7 + (lane < 7 ? lane : 0)) * FS;
    flag_wait<1>(ws, fho, tag);
    ST(10);
    __builtin_amdgcn_s_setprio(HPRIO);
    f16v acc[4];
#pragma unroll
    for (int k = 0; k < 4; k++) acc[k] = f16v{};
    {
      constexpr int DEPTH = 6;
      const u32 hs = WS_H + (tb * 2 * 112 + w * 28) * 1024;
      auto ldH = [&](int i, int tk) -> u4 { return ld128_dev(rs, lane * 16, hs + (tk * 112 + ord2(i)) * 1024); };
      const u32 tl = hr + (8 * (G >> 1) + qq) * 128 + 32 * (G & 1) + 8 * p;
      const u32 a0l = tl + 64 * (qq >> 1), a1l = tl + 64 * (1 - (qq >> 1));
      u4 B0[DEPTH], B1[DEPTH], an0, an1;
      u64 fb;
#pragma unroll
      for (int s = 0; s < DEPTH; s++) { B0[s] = ldH(s, 0); B1[s] = ldH(s, 1); }
      static_for<0, 28>([&](auto S) {
        constexpr int s = decltype(S)::value, r = s % DEPTH;
        const bf8 a0 = __builtin_bit_cast(bf8, s < NWR ? wr[s < NWR ? 2 * s : 0] : an0), a1 = __builtin_bit_cast(bf8, s < NWR ? wr[s < NWR ? 2 * s + 1 : 0] : an1);
        const bf8 b0 = __builtin_bit_cast(bf8, B0[r]), b1 = __builtin_bit_cast(bf8, B1[r]);
        if constexpr (s + 1 >= NWR && s + 1 < 28) {
          constexpr int o = (s + 1 - NWR) * 2048;
          an0 = cat(lds_tr(a0l + o), lds_tr(a0l + o + 512));
          an1 = cat(lds_tr(a1l + o), lds_tr(a1l + o + 512));
        }
        acc[0] = __builtin_amdgcn_mfma_f32_32x32x16_bf16(a0, b0, acc[0], 0, 0, 0);
        acc[2] = __builtin_amdgcn_mfma_f32_32x32x16_bf16(a1, b0, acc[2], 0, 0, 0);
        acc[1] = __builtin_amdgcn_mfma_f32_32x32x16_bf16(a0, b1, acc[1], 0, 0, 0);
        acc[3] = __builtin_amdgcn_mfma_f32_32x32x16_bf16(a1, b1, acc[3], 0, 0, 0);
        if constexpr (s + DEPTH == 10) fb = flag_load(ws, fho + 1024 * FS);
        if constexpr (s + DEPTH == 14) { SB(); if (__builtin_expect(!__all(fb == tag), 0)) flag_wait<1>(ws, fho + 1024 * FS, tag); asm volatile("" ::: "memory"); ST(19); }
        if constexpr (s + DEPTH < 28) { B0[r] = ldH(s + DEPTH, 0); B1[r] = ldH(s + DEPTH, 1); }
        SB();
      });
    }
    ST(11);
    f16v mine;
#pragma unroll
    for (int k = 0; k < 4; k++) {
      if (k == w) { mine = acc[k]; }
      else {
        const u32 a = hr + ((k - w - 1) & 3) * 4096 + lane * 16;
#pragma unroll
        for (int e = 0; e < 4; e++) lds_st<f4>(a + e * 1024, f4{acc[k][4 * e], acc[k][4 * e + 1], acc[k][4 * e + 2], acc[k][4 * e + 3]});
      }
    }
    WAIT_LGKM0();
    ST(12);
    TR(19);
    BAR();
    ST(13);
    {
      const u32 l16 = sm0 + L_XR + lane * 16;
      const u32 a0 = l16 + ((w + 3) & 3) * HREG, a1 = l16 + ((w + 2) & 3) * HREG + 4096, a2 = l16 + ((w + 1) & 3) * HREG + 8192;
      const u32 ab = scr(w ^ 1) + lane * 16;
      u2 o[4];
#pragma unroll
      for (int e = 0; e < 4; e++) {
        const f4 m4 = f4{mine[4 * e], mine[4 * e + 1], mine[4 * e + 2], mine[4 * e + 3]};
        const f4 v = ((lds_ld<f4>(a0 + e * 1024) + lds_ld<f4>(a1 + e * 1024)) + (lds_ld<f4>(a2 + e * 1024) + m4)) * r2 + lds_ld<f4>(ab + e * 1024);
        o[e] = u2{pk_bf16(v[0], v[1]), pk_bf16(v[2], v[3])};
      }
      u16* op = out + (size_t)(tb * 64 + (w & 1) * 32 + (lane & 31)) * D + cb * 64 + (w >> 1) * 32 + 8 * (lane >> 5);
#pragma unroll
      for (int e = 0; e < 4; e += 2) {
        auto r0 = __builtin_amdgcn_permlane32_swap(o[e][0], o[e + 1][0], false, false);
        auto r1 = __builtin_amdgcn_permlane32_swap(o[e][1], o[e + 1][1], false, false);
        *(u4*)(op + 8 * e) = u4{r0[0], r1[0], r0[1], r1[1]};
      }
    }
    ST(14);
  }
}

extern "C" void kernel_launch(void* const* d_in, const int* in_sizes, int n_in, void* d_out, int out_size,
                              void* d_ws, size_t ws_size, hipStream_t stream) {
  (void)hipFuncSetAttribute((const void*)fused, hipFuncAttributeMaxDynamicSharedMemorySize, LDS_BYTES);
  fused<<<256, 512, LDS_BYTES, stream>>>((const u16*)d_in[0], (const u16*)d_in[1], (const u16*)d_in[2], (const u16*)d_in[3],
                                         (const u16*)d_in[4], (const u16*)d_in[5], (const u16*)d_in[6], (const u16*)d_in[8],
                                         (const u16*)d_in[9], (const u16*)d_in[10], (char*)d_ws, (u16*)d_out);
}
```

```cpp
#include <hip/hip_runtime.h>
#include <hip/hip_bf16.h>

typedef unsigned short u16;
typedef unsigned int u32;
typedef unsigned long long u64;
typedef __attribute__((ext_vector_type(8))) __bf16 bf8;
typedef __attribute__((ext_vector_type(2))) __bf16 bf2;
typedef __attribute__((ext_vector_type(8))) _Float16 h8;
typedef __attribute__((ext_vector_type(2))) _Float16 h2;
typedef __attribute__((ext_vector_type(4))) u32 u4;
typedef __attribute__((ext_vector_type(2))) u32 u2;
typedef __attribute__((ext_vector_type(4))) short s4;
typedef __attribute__((ext_vector_type(16))) float f16v;
typedef __attribute__((ext_vector_type(4))) float f4;
typedef __attribute__((ext_vector_type(2))) float f2;
#define LAS __attribute__((address_space(3)))
#define GAS __attribute__((address_space(1)))
#define DEV __device__ __forceinline__

constexpr int PF_CODE = 21;
constexpr int ASLEEP = 8;
constexpr int HSLEEP = 8;
constexpr int YSLEEP = 48;
constexpr int SSLEEP = 24;
constexpr int PEEK_BACK = 4;
constexpr int DEP0 = 4, DEPM = 7, BSTEP = 8;
constexpr int PUBN = 84;

constexpr int D = 512, NM = 128;
constexpr u32 WS_H = 0;
constexpr u32 WS_WD = WS_H + 64 * 112 * 1024;
constexpr u32 WS_XT = WS_WD + 32 * 28 * 2048;
constexpr u32 WS_YT = WS_XT + 2 * 1024 * 1024;
constexpr u32 WS_YC = WS_YT + 2 * 1024 * 1024;
constexpr u32 WS_SS = WS_YC + 512 * 32 * 8;
constexpr u32 WS_CS = WS_SS + 2048 * 8;
constexpr u32 FS = 256;
constexpr u32 WS_FX = WS_CS + 14 * 16384;
constexpr u32 WS_FA = WS_FX + 32 * FS;
constexpr u32 WS_FS = WS_FA + 32 * FS;
constexpr u32 WS_FW = WS_FS + 32 * FS;
constexpr u32 WS_FC = WS_FW + 32 * FS;
constexpr u32 WS_FH = WS_FC + 256 * FS;
constexpr u32 WS_EP = WS_FH + 2 * 32 * 32 * FS;
constexpr u32 WS_END = WS_EP + 256;

constexpr int KSZ = 11520;
constexpr int L_KS = 0;
constexpr int L_XR = 4 * KSZ;
constexpr int L_WR = L_XR + 49152;
constexpr int L_CS = L_WR + 65536;
constexpr int L_MS = L_CS + 1024;
constexpr int L_MI = L_MS + 512;
constexpr int LDS_BYTES = L_MI + 64;
constexpr int HREG = 26624;
constexpr int NWR = 15;
constexpr int L_UB = L_WR + 16384;
constexpr int NW_EARLY = 20;
constexpr int PSTR = 1056;
constexpr int SPIN_CAP = 1 << 17;

DEV float blo(u32 v) { return __uint_as_float(v << 16); }
DEV float bhi(u32 v) { return __uint_as_float(v & 0xffff0000u); }
DEV u32 pk_bf16(float a, float b) { f2 v = {a, b}; return __builtin_bit_cast(u32, __builtin_convertvector(v, bf2)); }
DEV u32 pk_f16(float a, float b) { f2 v = {a, b}; return __builtin_bit_cast(u32, __builtin_convertvector(v, h2)); }
DEV u32 pk_f16_conj(f2 v) { u32 r; asm("v_cvt_pk_f16_f32 %0, %1, -%2" : "=v"(r) : "v"(v[0]), "v"(v[1])); return r; }
DEV bf2 as_bf2(u32 v) { return __builtin_bit_cast(bf2, v); }
DEV h2 as_h2(u32 v) { return __builtin_bit_cast(h2, v); }
DEV float rbf(float f) { return blo(pk_bf16(f, f)); }

DEV void set_m0(u32 lds) { asm volatile("s_mov_b32 m0, %0\n\ts_nop 0" ::"s"(lds) : "memory"); }
template <int OFF> DEV void dma_off(const void* sbase, u32 voff) {
  asm volatile("global_load_lds_dwordx4 %0, %1 offset:%2" ::"v"(voff), "s"(sbase), "n"(OFF) : "memory");
}
template <int OFF> DEV void lds_st_tid(u32 v) { asm volatile("ds_write_addtid_b32 %0 offset:%1" ::"v"(v), "n"(OFF) : "memory"); }
DEV u32 vm_pending() { u32 r; asm volatile("s_getreg_b32 %0, hwreg(HW_REG_IB_STS, 0, 4)" : "=s"(r)); return r; }
template <int OFF> DEV void dma_off_dev(const void* sbase, u32 voff) {
  asm volatile("global_load_lds_dwordx4 %0, %1 offset:%2 sc1" ::"v"(voff), "s"(sbase), "n"(OFF) : "memory");
}
template <int N> DEV void wait_vm() { asm volatile("s_waitcnt vmcnt(%0)" ::"n"(N) : "memory"); }
#define WAIT_LGKM0() asm volatile("s_waitcnt lgkmcnt(0)" ::: "memory")
#define SB() __builtin_amdgcn_sched_barrier(0)
#define BAR() do { SB(); asm volatile("s_barrier" ::: "memory"); SB(); } while (0)

template <int V> struct ic { static constexpr int value = V; };
template <int A, int B, class Fn> DEV void static_for(Fn&& f) {
  if constexpr (A < B) { f(ic<A>{}); static_for<A + 1, B>(f); }
}
template <class T> DEV T lds_ld(u32 a) { return *(LAS T*)(size_t)a; }
template <class T> DEV void lds_st(u32 a, T v) { *(LAS T*)(size_t)a = v; }
DEV u2 lds_tr(u32 a) { return __builtin_bit_cast(u2, __builtin_amdgcn_ds_read_tr16_b64_v4i16((LAS s4*)(size_t)a)); }
DEV u4 cat(u2 a, u2 b) { return u4{a[0], a[1], b[0], b[1]}; }

typedef __amdgpu_buffer_rsrc_t rsrc_t;
DEV u4 ld128_dev(rsrc_t r, u32 v, u32 s) { return __builtin_amdgcn_raw_buffer_load_b128(r, v, s, 16); }
DEV u2 ld64_dev(rsrc_t r, u32 v, u32 s) { return __builtin_amdgcn_raw_buffer_load_b64(r, v, s, 16); }
DEV void st128_dev(rsrc_t r, u32 v, u4 d) { __builtin_amdgcn_raw_buffer_store_b128(d, r, v, 0, 16); }
DEV void st64_dev(rsrc_t r, u32 v, u2 d) { __builtin_amdgcn_raw_buffer_store_b64(d, r, v, 0, 16); }
DEV void flag_store(char* ws, u32 off, u64 tag) {
  __hip_atomic_store((GAS u64*)(ws + off), tag, __ATOMIC_RELAXED, __HIP_MEMORY_SCOPE_AGENT);
}
DEV u64 flag_load(const char* ws, u32 off) {
  return __hip_atomic_load((GAS u64*)(ws + off), __ATOMIC_RELAXED, __HIP_MEMORY_SCOPE_AGENT);
}
template <int SLP> DEV void flag_wait(const char* ws, u32 off, u64 tag) {
  bool pend = true;
#pragma unroll 1
  for (int s = 0; s < SPIN_CAP; s++) {
    if (pend) pend = flag_load(ws, off) != tag;
    if (!__any(pend)) break;
    __builtin_amdgcn_s_sleep(SLP);
  }
  asm volatile("" ::: "memory");
}
DEV void flag_peek(u64& v, const char* ws, u32 off) {
  asm volatile("global_load_dwordx2 %0, %1, %2 sc1" : "=v"(v) : "v"(off), "s"(ws) : "memory");
}

DEV bool lds_arrive(u32 a, u32 n, int lane) {
  u32 old = 0;
  if (lane == 0) old = __hip_atomic_fetch_add((LAS u32*)(size_t)a, 1u, __ATOMIC_RELAXED, __HIP_MEMORY_SCOPE_WORKGROUP);
  return (u32)__builtin_amdgcn_readfirstlane(old) == n - 1;
}
DEV float xsum16(float v) { u32 u = __float_as_uint(v); auto r = __builtin_amdgcn_permlane16_swap(u, u, false, false); return __uint_as_float(r[0]) + __uint_as_float(r[1]); }
DEV float xsum32(float v) { u32 u = __float_as_uint(v); auto r = __builtin_amdgcn_permlane32_swap(u, u, false, false); return __uint_as_float(r[0]) + __uint_as_float(r[1]); }
template <int CTRL> DEV u32 xor_dpp(u32 v) { return v ^ (u32)__builtin_amdgcn_update_dpp(0, (int)v, CTRL, 0xf, 0xf, true); }
DEV u32 xxor32(u32 u) { auto r = __builtin_amdgcn_permlane32_swap(u, u, false, false); return r[0] ^ r[1]; }
DEV u32 xmax16(u32 u) { auto r = __builtin_amdgcn_permlane16_swap(u, u, false, false); return max(r[0], r[1]); }
DEV u32 xmax32(u32 u) { auto r = __builtin_amdgcn_permlane32_swap(u, u, false, false); return max(r[0], r[1]); }

template <bool FIRST> DEV void mma_bf16(f4& c, u4 a, u4 b) {
  if constexpr (FIRST) asm volatile("v_mfma_f32_16x16x32_bf16 %0, %1, %2, 0" : "=&v"(c) : "v"(a), "v"(b));
  else asm volatile("v_mfma_f32_16x16x32_bf16 %0, %1, %2, %0" : "+v"(c) : "v"(a), "v"(b));
}
template <bool FIRST> DEV void mma_f16(f4& c, u4 a, u4 b) {
  if constexpr (FIRST) asm volatile("v_mfma_f32_16x16x32_f16 %0, %1, %2, 0" : "=&v"(c) : "v"(a), "v"(b));
  else asm volatile("v_mfma_f32_16x16x32_f16 %0, %1, %2, %0" : "+v"(c) : "v"(a), "v"(b));
}
template <bool FIRST> DEV void mma32_bf16(f16v& c, u4 a, u4 b) {
  if constexpr (FIRST) asm volatile("v_mfma_f32_32x32x16_bf16 %0, %1, %2, 0" : "=&v"(c) : "v"(a), "v"(b));
  else asm volatile("v_mfma_f32_32x32x16_bf16 %0, %1, %2, %0" : "+v"(c) : "v"(a), "v"(b));
}
DEV void mfma_done() { SB(); asm volatile("s_nop 7"); SB(); }
DEV f2 cmul_pk(f2 a, f2 b) {
  f2 t;
  asm("v_pk_mul_f32 %0, %1, %2 op_sel_hi:[0,1]\n\tv_pk_fma_f32 %0, %1, %2, %0 op_sel:[1,1,0] op_sel_hi:[1,0,1] neg_lo:[0,1,0]"
      : "=&v"(t) : "v"(a), "v"(b));
  return t;
}
DEV void cmul_pk2(f2& o0, f2& o1, f2 a0, f2 b0, f2 a1, f2 b1) {
  asm("v_pk_mul_f32 %0, %2, %3 op_sel_hi:[0,1]\n\tv_pk_mul_f32 %1, %4, %5 op_sel_hi:[0,1]\n\t"
      "v_pk_fma_f32 %0, %2, %3, %0 op_sel:[1,1,0] op_sel_hi:[1,0,1] neg_lo:[0,1,0]\n\t"
      "v_pk_fma_f32 %1, %4, %5, %1 op_sel:[1,1,0] op_sel_hi:[1,0,1] neg_lo:[0,1,0]"
      : "=&v"(o0), "=&v"(o1) : "v"(a0), "v"(b0), "v"(a1), "v"(b1));
}
struct cf { float r, i; };
DEV float mul1(float a, float b) { float t; asm("v_mul_f32 %0, %1, %2" : "=v"(t) : "v"(a), "v"(b)); return t; }
DEV float fma1(float a, float b, float c) { float t; asm("v_fma_f32 %0, %1, %2, %3" : "=v"(t) : "v"(a), "v"(b), "v"(c)); return t; }
DEV float add1(float a, float b) { float t; asm("v_add_f32 %0, %1, %2" : "=v"(t) : "v"(a), "v"(b)); return t; }
DEV cf cexp_at(float lr, float fr, float p) {
  float e = __builtin_amdgcn_exp2f(lr * p);
  float ph = __builtin_amdgcn_fractf(fr * p);
  float c = __builtin_amdgcn_cosf(ph), s = __builtin_amdgcn_sinf(ph);
  asm("s_nop 0" : "+v"(e), "+v"(c), "+v"(s));
  return cf{mul1(e, c), mul1(e, s)};
}
struct prm { u32 lar, aim, ldt, dsk, cr, ci, cr2, ci2; u64 ep; };
DEV void prm_load(prm& q, u32 o1, u32 o3, const u16* lar, const u16* aim, const u16* cr, const u16* ci, const u16* ldt, const u16* dsk, const char* ws) {
  asm volatile("global_load_ushort %0, %9, %13\n\tglobal_load_ushort %1, %9, %14\n\tglobal_load_ushort %2, %11, %17\n\t"
               "global_load_ushort %4, %9, %15\n\tglobal_load_ushort %5, %9, %16\n\tglobal_load_ushort %6, %10, %15\n\t"
               "global_load_ushort %7, %10, %16\n\tglobal_load_ushort %3, %11, %18\n\tglobal_load_dwordx2 %8, %12, %19 sc1"
               : "=&v"(q.lar), "=&v"(q.aim), "=&v"(q.ldt), "=&v"(q.dsk), "=&v"(q.cr), "=&v"(q.ci), "=&v"(q.cr2), "=&v"(q.ci2), "=&v"(q.ep)
               : "v"(o1), "v"(o1 ^ 128), "v"(o3), "v"(WS_EP), "s"(lar), "s"(aim), "s"(cr), "s"(ci), "s"(ldt), "s"(dsk), "s"(ws) : "memory");
}
DEV void prm_wait(prm& q) {
  asm volatile("s_waitcnt vmcnt(0)" : "+v"(q.lar), "+v"(q.aim), "+v"(q.ldt), "+v"(q.dsk), "+v"(q.cr), "+v"(q.ci), "+v"(q.cr2), "+v"(q.ci2), "+v"(q.ep) :: "memory");
}
DEV u32 cidx_hi(int l) { return (pk_bf16((float)l, 0.f) & 0xffffu) - 17024u; }

struct grp { int t, n, by; };
constexpr grp SCHED[] = {
  {-1, 4, 0}, {-1, 4, 0}, {-1, 4, 1}, {-1, 4, 1}, {-1, 1, 2},
  {-1, 4, 2},
  {0, 4, 3}, {0, 2, 3},
  {1, 2, 3}, {1, 4, 4}, {1, 2, 5},
  {2, 2, 5}, {2, 4, 5},
  {3, 1, 6},
  {4, 4, 6}, {4, 4, 7},
  {5, 4, 7}, {5, 4, 8},
  {6, 1, 99}, {6, 4, 9}, {6, 4, 9},
  {7, 2, 11}, {7, 4, 10},
  {8, 2, 11}, {8, 4, 11},
  {9, 2, 12}, {9, 4, 12},
  {10, 1, 12}, {10, 2, 12}, {10, 4, 12},
  {11, 4, 13}, {11, 4, 14},
  {12, 6, 99},
  {13, 4, 99},
};
constexpr int n_wait(int bar) {
  int n = 0;
  for (const grp& g : SCHED) {
    if (g.t >= bar) break;
    n = g.by <= bar ? 0 : n + g.n;
  }
  return n;
}
__host__ __device__ constexpr int seq_t(int i) { return i < 96 ? i >> 3 : 12 + (((i - 96) >> 2) & 3); }
__host__ __device__ constexpr int seq_n(int i) { return i < 96 ? i & 7 : ((i - 96) & 3) + (i >= 112 ? 4 : 0); }
__host__ __device__ constexpr int w_slot(int t) {
  constexpr int sl[15] = {2, 3, 1, 0, 2, 3, 0, 2, 1, 3, 0, 2, 1, 3, 0};
  return t == 15 ? L_XR + 2 * 16384 : L_WR + sl[t] * 16384;
}
__host__ __device__ constexpr int h_req(int s) {
  const int d = DEP0 + s < DEPM ? DEP0 + s : DEPM, cap = s <= BSTEP ? 14 : 14 + 2 * (s - BSTEP);
  const int r = s + d < cap ? s + d : cap;
  return r < 28 ? r : 28;
}
__host__ __device__ constexpr int pos2(int kk) { return ((kk >> 1) & 1) * 14 + (kk >> 2) * 2 + (kk & 1); }

__global__ __launch_bounds__(512, 2) void fused(const u16* __restrict__ x, const u16* __restrict__ lar,
                                                const u16* __restrict__ aim, const u16* __restrict__ cr,
                                                const u16* __restrict__ ci, const u16* __restrict__ ldt,
                                                const u16* __restrict__ dsk, const u16* __restrict__ wg,
                                                const u16* __restrict__ wu, const u16* __restrict__ wd,
                                                char* __restrict__ ws, u16* __restrict__ out) {
  extern __shared__ __attribute__((aligned(16))) char sm[];
  const u32 sm0 = (u32)(size_t)(LAS char*)sm;
  const int b = blockIdx.x, xcd = b & 7, qi = b >> 6, qj = (b >> 3) & 7;
  const bool isS = qj == 7;
  const int ti = (xcd >> 1) * 4 + qi, tj = (xcd & 1) * 7 + qj;
  const int tb = xcd * 4 + qi, cb = qj;
  const int wv = __builtin_amdgcn_readfirstlane(threadIdx.x >> 6);
  const int w = wv & 3;
  const int chl = w >> 1, hw = w & 1, ch = (xcd * 32 + (b >> 3)) * 2 + chl;
  const u32 ks = sm0 + L_KS + w * KSZ;
  const u32 xs = sm0 + L_KS + (2 * chl + 1) * KSZ, Pt = sm0 + L_KS + 2 * chl * KSZ;
  auto scr = [&](int v) -> u32 {
    const u32 a = L_KS + v * KSZ + 4608, c = L_XR + 4 * HREG + (v >> 1) * 4096, m = -(u32)(v & 1);
    return sm0 + ((a & m) | (c & ~m));
  };
#define LANE_VARS                                                                                                     \
  int tid_ = threadIdx.x;                                                                                             \
  asm volatile("" : "+v"(tid_));                                                                                      \
  const int lane = tid_ & 63;                                                                                         \
  [[maybe_unused]] const int G = lane >> 4, qq = (lane & 15) >> 2, p = lane & 3, a16 = lane & 15;                     \
  [[maybe_unused]] const u32 ve = (lane >> 3) * 1024 + (((lane & 7) ^ (lane >> 4)) << 4);                         \
  [[maybe_unused]] const u32 vd = (7 + (lane >> 3)) * 1024 + (((lane & 7) ^ (4 + (lane >> 4))) << 4);
  auto out_off = [&](int lane) -> u32 { return ((tb * 64 + wv * 8 + (lane >> 3)) * D + cb * 64 + (lane & 7) * 8) * 2; };
  struct fin { u32 pa, pb, ya, ra, oo; f4 xa, xb; };
  auto finish_prep = [&](int lane, u4 xq) -> fin {
    fin f;
    const int t = wv * 8 + (lane >> 3), g8 = lane & 7, sw = (g8 >> 1) ^ 1;
    f.pa = sm0 + L_XR + (g8 + 8 * (t >> 5)) * PSTR + (t & 31) * 16;
    f.pb = f.pa + 2 * HREG;
    f.ya = sm0 + ((sw & 1) ? L_KS + sw * KSZ + 4608 : L_XR + 4 * HREG + (sw >> 1) * 4096) + 2048 + t * 32 + (g8 & 1) * 16;
    f.ra = sm0 + L_MS + t * 4;
    f.oo = out_off(lane);
    f.xa = f4{blo(xq[0]), bhi(xq[0]), blo(xq[1]), bhi(xq[1])};
    f.xb = f4{blo(xq[2]), bhi(xq[2]), blo(xq[3]), bhi(xq[3])};
    asm volatile("" : "+v"(f.pa), "+v"(f.pb), "+v"(f.ya), "+v"(f.ra), "+v"(f.oo), "+v"(f.xa), "+v"(f.xb));
    return f;
  };
  auto finish = [&](const fin& f) {
    const u4 yq = lds_ld<u4>(f.ya);
    const float r2 = lds_ld<float>(f.ra);
    f4 pv[8];
#pragma unroll
    for (int i = 0; i < 8; i++) pv[i] = lds_ld<f4>(((i & 2) ? f.pb : f.pa) + (i & 1) * HREG + (i >> 2) * 512);
    SB();
    u4 o;
#pragma unroll
    for (int h = 0; h < 2; h++) {
      const f4 bs = (h ? f.xb : f.xa) + f4{blo(yq[2 * h]), bhi(yq[2 * h]), blo(yq[2 * h + 1]), bhi(yq[2 * h + 1])};
      const f4 r = ((pv[4 * h] + pv[4 * h + 1]) + (pv[4 * h + 2] + pv[4 * h + 3])) * r2 + bs;
      o[2 * h] = pk_bf16(r[0], r[1]); o[2 * h + 1] = pk_bf16(r[2], r[3]);
    }
    __builtin_amdgcn_raw_buffer_store_b128(o, __builtin_amdgcn_make_buffer_rsrc(out, 0, 2048 * D * 2, 0x00020000), f.oo, 0, 16);
  };
  {
    u64 pc; u32 len;
    asm volatile("s_getpc_b64 %0\nPFL_%=:\n\ts_mov_b32 %1, FUSED_TAIL-PFL_%=" : "=s"(pc), "=s"(len));
    const u32 o = min((u32)((((b >> 3) & 15) * PF_CODE + threadIdx.x) * 128), len - 4u) & ~3u;
    set_m0(sm0 + L_MS);
    if (threadIdx.x < PF_CODE) asm volatile("global_load_lds_dword %0, %1" :: "v"(o), "s"(pc) : "memory");
  }
  const int rot = 2 * (xcd >> 1);
  prm pg;
  {
    LANE_VARS
    const u32 w0 = G * 3584 + ((a16 ^ (2 * G)) << 4), w2 = (8 + G) * 3584 + ((a16 ^ (2 * (G | 4))) << 4) - 2048;
    const u32 xo = (ti * 128 + w * 32) * 1024 + rot * 128, wo = (16 * (w & 1) + 64 * rot) * 3584 + tj * 256;
    SB();
    asm volatile("" :: "s"(x), "s"(lar), "s"(aim), "s"(cr), "s"(ci), "s"(ldt), "s"(dsk), "s"(wg), "s"(wu), "s"(wd), "s"(ws), "s"(out));
    if (isS) {
    } else if (wv < 4) {
      prm_load(pg, (ch * NM + hw * 64 + lane) * 2, ch * 2, lar, aim, cr, ci, ldt, dsk, ws);
    } else {
      const char* xs0 = (const char*)x + xo;
      const char* ws0 = (const char*)((w & 2) ? wu : wg) + wo;
      set_m0(sm0 + L_XR + w * 4096);
      dma_off<0>(xs0, ve); dma_off<1024>(xs0, vd); dma_off<2048>(xs0, ve + 14336); dma_off<3072>(xs0, vd + 14336);
      set_m0(sm0 + w_slot(0) + w * 4096);
      dma_off<0>(ws0, w0); dma_off<1024>(ws0, w0 + 4 * 3584 - 1024); dma_off<2048>(ws0, w2); dma_off<3072>(ws0, w2 + 4 * 3584 - 1024);
    }
    SB();
    if (wv + 8 * qj >= 56) {
      if (wv < 4) {
        if (lane == 0) { lds_st<u32>(sm0 + L_MI + 32 + w * 4, 0u); lds_st<u32>(sm0 + L_MI + 48 + w * 4, 0u); }
        const char* xb = (const char*)x + (tb * 64 + w * 16) * 1024;
#pragma unroll
        for (int c = 0; c < 8; c++) { set_m0(sm0 + L_WR + w * 16640 + c * 2048); dma_off<0>(xb + c * 128, ve); dma_off<1024>(xb + c * 128, vd); }
      } else {
        const u32 vo = (lane >> 3) * 1024 + (lane & 7) * 16;
#pragma unroll
        for (int i = 0; i < 7; i++) {
          const int u = tb * 7 + i;
          const char* src = (const char*)wd + (u >> 1) * 16384 + (u & 1) * 512 + w * 128;
          set_m0(sm0 + w * 14336 + i * 2048);
          dma_off<0>(src, vo);
          dma_off<1024>(src, vo + 7168);
        }
      }
    }
    SB();
  }

  if (wv >= 4) {
    LANE_VARS
    const rsrc_t rs = __builtin_amdgcn_make_buffer_rsrc(ws, 0, WS_END, 0x00020000);
    u4 vb[8], ua[8], cw[4], cso;
    f16v cs;
    float ev[32], kprev;
    u32 ea, pk[16];
    f16v dk;
    const int r32 = lane & 31;
    const u32 fra = ks + r32 * 272 + (lane >> 5) * 16;
    const u32 fru = (r32 < 15 ? sm0 + L_UB + w * 4096 + r32 * 272 : ks + 8704 + (min(r32, 20) - 15) * 272) + (lane >> 5) * 16;
    auto kop = [&](auto I) {
      constexpr int i = decltype(I)::value;
      if constexpr (i >= 60) {
        constexpr int z = i - 60;
        float k = add1(ev[2 * z], ev[2 * z + 1]);
        asm("s_nop 0" : "+v"(k));
        float old;
        if constexpr (z == 0) old = hw ? k : 0.f;
        else old = __int_as_float(__builtin_amdgcn_update_dpp(0, __float_as_int(kprev), 0x13C, 0xf, 0xf, false));
        const float km = __int_as_float(__builtin_amdgcn_update_dpp(__float_as_int(old), __float_as_int(k), 0x138, 0xf, 0xf, false));
        kprev = k;
        pk[z] = pk_f16(k, km);
      } else if constexpr (i >= 40) {
        constexpr int z = i - 40;
        if constexpr ((z & 3) == 0) {
          ea = cidx_hi(1024 * hw + 64 * z + lane);
          if constexpr (z == 0) ea = hw ? ea : (u32)lane;
          ea = sm0 + L_KS + 2 * chl * KSZ + 8704 + ea * 4;
        } else {
          ea += hw ? 32u : (z < 4 ? 256u : z < 8 ? 128u : 64u);
        }
        ev[2 * z] = lds_ld<float>(ea); ev[2 * z + 1] = lds_ld<float>(ea + KSZ);
      } else if constexpr (i == 30) {
        u4 sel;
#pragma unroll
        for (int e = 0; e < 4; e++) sel[e] = r32 == 2 * e ? 0x00003f80u : r32 == 2 * e + 1 ? 0x3f800000u : 0u;
        cs = f16v{};
        SB();
        __builtin_amdgcn_s_setprio(3);
#pragma unroll
        for (int ii = 0; ii < 4; ii++) cs = __builtin_amdgcn_mfma_f32_32x32x16_bf16(__builtin_bit_cast(bf8, sel), __builtin_bit_cast(bf8, cw[ii]), cs, 0, 0, 0);
        __builtin_amdgcn_s_setprio(0);
      } else if constexpr (i > 30) {
        const u32 c0 = __float_as_uint(cs[i - 31]);
        auto r = __builtin_amdgcn_permlane16_swap(c0, c0, false, false);
        float v = add1(__uint_as_float(r[0]), __uint_as_float(r[1]));
        asm("s_nop 1" : "+v"(v));
        cso[i - 31] = __float_as_uint(add1(v, __int_as_float(__builtin_amdgcn_update_dpp(0, __float_as_int(v), 0x128, 0xf, 0xf, false))));
      } else if constexpr (i < 8) ua[i] = lds_ld<u4>(fru + i * 32);
      else if constexpr (i < 16) vb[i - 8] = lds_ld<u4>(fra + (i - 8) * 32);
      else {
        if constexpr (i == 16) dk = f16v{};
        __builtin_amdgcn_s_setprio(3);
        dk = __builtin_amdgcn_mfma_f32_32x32x16_f16(__builtin_bit_cast(h8, ua[i - 16]), __builtin_bit_cast(h8, vb[i - 16]), dk, 0, 0, 0);
        __builtin_amdgcn_s_setprio(0);
      }
      SB();
    };
    auto put_ct = [&]() {
      const u32 ct = ks + 8704;
      const int h = lane >> 5;
      lds_st<f4>(ct + (8 * r32 + 4 * h) * 4, f4{dk[0], dk[1], dk[2], dk[3]});
      const int i1 = h ? (384 + 2 * r32) : (256 + 4 * r32), i2 = h ? (448 + 2 * r32) : (258 + 4 * r32);
      lds_st<f2>(ct + i1 * 4, f2{dk[4], dk[5]});
      lds_st<f2>(ct + i2 * 4, f2{dk[6], dk[7]});
      lds_st<float>(ct + (512 + 128 * h + r32) * 4, dk[8]);
      lds_st<float>(ct + (544 + 128 * h + r32) * 4, dk[9]);
      if (h == 0) { lds_st<float>(ct + (576 + r32) * 4, dk[10]); lds_st<float>(ct + (608 + r32) * 4, dk[11]); }
    };
    auto put_p = [&]() {
      const u32 pw = Pt + (2047 - 1024 * hw - 960 - lane) * 4;
#pragma unroll
      for (int z = 0; z < 16; z++) lds_st<u32>(pw + (15 - z) * 256, pk[z]);
      if (!hw && lane < 34) lds_st<u4>(lane < 30 ? xs + lane * 16 : Pt + 8192 + (lane - 30) * 16, u4{0, 0, 0, 0});
    };
    u4 wr[2 * NWR];
    const u32 wso = WS_WD + (cb * 4 + w) * 56 * 1024;
    const u32 fwo = WS_FW + (w * 8 + (lane & 7)) * FS;
    auto ldW = [&](int i) { wr[i] = ld128_dev(rs, lane * 16, wso + i * 1024); };
    u64 tag;
    float r2;
    u4 gx0, gx1;
    auto gather_ld = [&]() {
      const u32 so = WS_XT + ((ch >> 2) * 128 + hw * 64) * 128 + (ch & 3) * 32, vo = (lane >> 1) * 128 + (lane & 1) * 16;
      gx0 = ld128_dev(rs, vo, so); gx1 = ld128_dev(rs, vo, so + 4096);
      r2 = __uint_as_float(__builtin_amdgcn_raw_buffer_load_b32(rs, ((w & 1) * 32 + (lane & 31)) * 8 + 4, WS_SS + tb * 512, 16));
    };
    auto gather_st = [&]() {
      lds_st<u4>(xs + 480 + hw * 2048 + lane * 16, gx0);
      lds_st<u4>(xs + 480 + hw * 2048 + 1024 + lane * 16, gx1);
    };
    if (!isS) {
      const char* xb = (const char*)x + (size_t)(ti * 128 + w * 32) * 1024;
      const char* wb = (const char*)((w & 2) ? wu : wg) + (size_t)(16 * (w & 1)) * 3584 + tj * 256;
      u32 wvo[4];
#pragma unroll
      for (int ii = 0; ii < 4; ii++) wvo[ii] = (4 * ii + G) * 3584 + ((a16 ^ (2 * (G | ((ii >> 1) << 2)))) << 4) - ii * 1024;
      auto fill = [&](auto K, auto E) { constexpr int k = decltype(K)::value; if constexpr (k >= 0 && k < decltype(E)::value) kop(ic<k>{}); };
      auto dmaX = [&](auto S, auto HALF, auto K, auto E) {
        constexpr int s = decltype(S)::value, half = decltype(HALF)::value, k = decltype(K)::value;
        const char* src = xb + ((s + rot) & 7) * 128 + half * 16384;
        set_m0(sm0 + L_XR + (s % 3) * 16384 + w * 4096 + half * 2048);
        dma_off<0>(src, ve); fill(ic<k>{}, E);
        dma_off<1024>(src, vd); fill(ic<(k < 0 ? -1 : k + 1)>{}, E);
      };
      auto dmaW = [&](auto T, auto K, auto E) {
        constexpr int t = decltype(T)::value, k = decltype(K)::value;
        const char* src = wb + (size_t)((t + 2 * rot) & 15) * (32 * 3584);
        set_m0(sm0 + w_slot(t) + w * 4096);
        dma_off<0>(src, wvo[0]); fill(ic<k>{}, E);
        dma_off<1024>(src, wvo[1]); fill(ic<(k < 0 ? -1 : k + 1)>{}, E);
        dma_off<2048>(src, wvo[2]); fill(ic<(k < 0 ? -1 : k + 2)>{}, E);
        dma_off<3072>(src, wvo[3]); fill(ic<(k < 0 ? -1 : k + 3)>{}, E);
      };
      using N = ic<-1>;
      dmaW(ic<1>{}, N{}, N{}); dmaX(ic<1>{}, ic<0>{}, N{}, N{}); dmaX(ic<1>{}, ic<1>{}, N{}, N{});
      u64 ep;
      flag_peek(ep, ws, WS_EP);
      dmaW(ic<2>{}, N{}, N{});
      wait_vm<n_wait(0)>();
      BAR();
      u64 f1, f2_;
      u4 cpart[4];
      u32 mval;
      const u32 fco = lane == 16 ? WS_FS + (2 * ti + (w >> 1)) * FS : lane >= 32 && lane < 40 ? fwo : WS_FC + (tj * 16 + 4 * w + (lane & 3)) * FS;
      const u32 fxo = WS_FX + (8 * w + (lane & 7)) * FS;
      static_for<0, 15>([&](auto T) {
        constexpr int t = decltype(T)::value;
        if constexpr (t < 4) {
          if (qi == t) {
            const u32 cra = sm0 + w_slot(t) + (w >> 1) * 8192 + (lane >> 3) * 256;
#pragma unroll
            for (int ii = 0; ii < 4; ii++)
              cw[ii] = lds_ld<u4>(cra + ii * 2048 + ((((w & 1) * 8 + (lane & 7)) ^ (2 * (((lane >> 3) & 3) | ((ii & 1) << 2)))) << 4));
          }
          SB();
        }
        if constexpr (t == 0) { dmaW(ic<3>{}, N{}, N{}); dmaX(ic<2>{}, ic<0>{}, N{}, N{}); }
        if constexpr (t == 1) { dmaX(ic<2>{}, ic<1>{}, N{}, N{}); dmaW(ic<4>{}, N{}, N{}); dmaX(ic<3>{}, ic<0>{}, N{}, N{}); }
        if constexpr (t == 2) { dmaX(ic<3>{}, ic<1>{}, N{}, N{}); dmaW(ic<5>{}, N{}, N{}); }
        if constexpr (t == 2) { asm volatile("" : "+v"(ep) :: "memory"); tag = ep + 1; }
        if constexpr (t == 3) {
          static_for<30, 35>(kop);
          if (r32 < 8) st128_dev(rs, WS_CS + (tj * 16 + ti) * 1024 + w * 256 + r32 * 32 + (lane >> 5) * 16, cso);
        }
        auto ldWk = [&](int i, auto K) { ldW(i); SB(); kop(K); };
        if constexpr (t == 4) { dmaW(ic<6>{}, N{}, N{}); dmaX(ic<4>{}, ic<0>{}, N{}, N{}); dmaX(ic<4>{}, ic<1>{}, N{}, N{}); }
        if constexpr (t == 6) { if (lane == 0) flag_store(ws, WS_FC + (tj * 16 + ti) * FS, tag); }
        if constexpr (t == 5) {
          static_for<0, 4>(kop);
          dmaW(ic<7>{}, ic<4>{}, ic<8>{});
          WAIT_LGKM0();
          dmaW(ic<8>{}, N{}, N{});
        }
        if constexpr (t == 6) { dmaX(ic<5>{}, ic<0>{}, ic<8>{}, ic<16>{}); dmaX(ic<5>{}, ic<1>{}, ic<10>{}, ic<16>{}); dmaW(ic<9>{}, ic<12>{}, ic<16>{}); }
        if constexpr (t == 7) {
          dmaX(ic<6>{}, ic<0>{}, ic<16>{}, ic<24>{}); dmaW(ic<10>{}, ic<18>{}, ic<24>{});
          kop(ic<22>{}); kop(ic<23>{});
        }
        if constexpr (t == 8) {
          __builtin_amdgcn_s_setprio(3);
          put_ct();
          __builtin_amdgcn_s_setprio(0);
          SB();
          dmaX(ic<6>{}, ic<1>{}, N{}, N{}); dmaW(ic<11>{}, N{}, N{});
        }
        if constexpr (t == 9) {
          dmaX(ic<7>{}, ic<0>{}, ic<40>{}, ic<56>{}); dmaW(ic<12>{}, ic<42>{}, ic<56>{}); kop(ic<46>{}); kop(ic<47>{});
        }
        if constexpr (t == 10) { flag_peek(f1, ws, fco); dmaX(ic<7>{}, ic<1>{}, ic<48>{}, ic<56>{}); dmaW(ic<13>{}, ic<50>{}, ic<56>{}); kop(ic<54>{}); kop(ic<55>{}); }
        if constexpr (t == 11) {
          dmaW(ic<14>{}, ic<60>{}, ic<76>{}); dmaW(ic<15>{}, ic<64>{}, ic<76>{});
          asm volatile("s_waitcnt vmcnt(14)" : "+v"(f1) :: "memory");
          if (__builtin_expect(!__all(f1 == tag), 0)) flag_wait<1>(ws, fco, tag);
        }
        if constexpr (t == 12) {
          static_for<0, 4>([&](auto I) {
            constexpr int i = decltype(I)::value;
            cpart[i] = ld128_dev(rs, G * 1024 + a16 * 16, WS_CS + (tj * 16 + 4 * i) * 1024 + w * 256);
            SB();
            kop(ic<68 + i>{});
          });
          mval = __builtin_amdgcn_raw_buffer_load_b32(rs, (lane & 31) * 8, WS_SS + (ti * 128 + w * 32) * 8, 16);
          f2_ = flag_load(ws, fxo);
        }
        if constexpr (t == 13) { ldWk(0, ic<72>{}); ldWk(1, ic<73>{}); ldWk(2, ic<74>{}); ldWk(3, ic<75>{}); }
        if constexpr (t == 14) {
#pragma unroll
          for (int i = 4; i < 8; i++) ldW(i);
          SB();
          f4 c4;
#pragma unroll
          for (int e = 0; e < 4; e++) {
            const u32 s4_ = __float_as_uint(add1(add1(__uint_as_float(cpart[0][e]), __uint_as_float(cpart[1][e])),
                                                 add1(__uint_as_float(cpart[2][e]), __uint_as_float(cpart[3][e]))));
            auto r = __builtin_amdgcn_permlane16_swap(s4_, s4_, false, false);
            const u32 v = __float_as_uint(add1(__uint_as_float(r[0]), __uint_as_float(r[1])));
            auto r2_ = __builtin_amdgcn_permlane32_swap(v, v, false, false);
            c4[e] = add1(__uint_as_float(r2_[0]), __uint_as_float(r2_[1]));
          }
          SB();
          __builtin_amdgcn_s_setprio(3);
          lds_st<f4>(sm0 + L_CS + w * 256 + a16 * 16, c4);
          lds_st<u32>(sm0 + L_MS + (w * 32 + (lane & 31)) * 4, mval);
          __builtin_amdgcn_s_setprio(0);
          if (__builtin_expect(!__all(f2_ == tag), 0)) flag_wait<1>(ws, fxo, tag);
        }
        if constexpr (t <= 13) wait_vm<n_wait(t + 1)>();
        WAIT_LGKM0();
        BAR();
      });
      gather_ld();
      put_p();
      __builtin_amdgcn_s_sleep(ASLEEP);
#pragma unroll
      for (int i = 8; i < NW_EARLY; i++) ldW(i);
      SB();
      __builtin_amdgcn_s_setprio(3);
      gather_st();
      __builtin_amdgcn_s_setprio(0);
      WAIT_LGKM0();
    } else {
      u64 ep;
      flag_peek(ep, ws, WS_EP);
      const u32 bo = sm0 + w * 14336;
      BAR();
      {
        const u32 ra = bo + (8 * (G >> 1) + qq) * 128 + 32 * (G & 1) + 8 * p;
        static_for<0, 7>([&](auto I) {
          constexpr int i = decltype(I)::value;
          wait_vm<13>();
          const int u = tb * 7 + i, kk = (u >> 1) - (tb >> 3) * 28;
          const u32 o = WS_WD + (((((u & 1) * 4 + w) * 4 + (tb >> 3)) * 28 + pos2(kk)) * 2) * 1024 + lane * 16;
          st128_dev(rs, o, cat(lds_tr(ra + i * 2048), lds_tr(ra + i * 2048 + 512)));
          st128_dev(rs, o + 1024, cat(lds_tr(ra + i * 2048 + 64), lds_tr(ra + i * 2048 + 64 + 512)));
          SB();
        });
      }
      asm volatile("s_waitcnt vmcnt(0)" : "+v"(ep) :: "memory");
      tag = ep + 1;
      if (lds_arrive(sm0 + L_MI + 52, 4, lane) && lane == 0) flag_store(ws, WS_FW + tb * FS, tag);
      WAIT_LGKM0();
      BAR();
      BAR();
      static_for<0, 24>(kop);
      put_ct();
      WAIT_LGKM0();
      BAR();
      static_for<40, 56>(kop);
      static_for<60, 76>(kop);
      put_p();
      flag_wait<4>(ws, lane < 32 ? WS_FX + lane * FS : fwo, tag);
      gather_ld();
#pragma unroll
      for (int i = 0; i < 8; i++) ldW(i);
      gather_st();
      WAIT_LGKM0();
    }
    BAR();
    if (!isS) {
      __builtin_amdgcn_s_sleep(HSLEEP);
    } else {
#pragma unroll
      for (int i = 8; i < NW_EARLY; i++) ldW(i);
    }
#pragma unroll
    for (int i = NW_EARLY; i < 2 * NWR; i++) ldW(i);
    const u32 hr = sm0 + L_XR + w * HREG;
    const u32 fho = WS_FH + (tb * 32 + w * 7 + (lane < 7 ? lane : 0)) * FS;
    u64 fa;
    {
      const char* src = ws + wso;
      static_for<NWR, 28>([&](auto I) {
        constexpr int i = decltype(I)::value;
        set_m0(hr + (i - NWR) * 2048);
        dma_off_dev<0>(src + i * 2048, lane * 16);
        dma_off_dev<1024>(src + i * 2048, lane * 16);
        if constexpr (i == 27 - PEEK_BACK) flag_peek(fa, ws, fho);
      });
    }
    asm volatile("s_waitcnt vmcnt(%1)" : "+v"(fa) : "n"(2 * PEEK_BACK) : "memory");
    if (__builtin_expect(!__all(fa == tag), 0)) flag_wait<1>(ws, fho, tag);
    asm volatile("" ::: "memory");
    f16v acc[4];
    u4 xq;
    fin fz;
    {
      const u32 hs = WS_H + (tb * 2 * 112 + w * 28) * 1024;
      u4 B0[28], B1[28], an0, an1;
      u64 fb;
      auto ldH = [&](auto I, int tk) {
        constexpr int i = decltype(I)::value;
        if constexpr (i == 14) { if (tk == 0) { if (__builtin_expect(!__all(fb == tag), 0)) flag_wait<1>(ws, fho + 1024 * FS, tag); asm volatile("" ::: "memory"); } }
        (tk ? B1 : B0)[i] = ld128_dev(rs, lane * 16, hs + (tk * 112 + i) * 1024);
        SB();
      };
      const u32 al = hr + lane * 16;
      static_for<0, h_req(0)>([&](auto I) { ldH(I, 0); ldH(I, 1); });
      auto req = [&](auto R) {
        constexpr int r = decltype(R)::value;
        if constexpr (r < 50) ldH(ic<(r >> 1)>{}, r & 1);
        else ldH(ic<25 + (r - 50) % 3>{}, (r - 50) / 3);
      };
      u4 aw[6];
      static_for<0, 25>([&](auto S) {
        constexpr int s = decltype(S)::value, n0 = 2 * h_req(s), nn = h_req(s + 1) - h_req(s);
        const u4 a0 = s < NWR ? wr[s < NWR ? 2 * s : 0] : an0, a1 = s < NWR ? wr[s < NWR ? 2 * s + 1 : 0] : an1;
        constexpr bool nx = s + 1 >= NWR && s + 1 < 25;
        constexpr int o = (s + 1 - NWR) * 2048;
        mma32_bf16<s == 0>(acc[0], a0, B0[s]);
        SB();
        if constexpr (s == BSTEP - 5) { fb = flag_load(ws, fho + 1024 * FS); SB(); }
        if constexpr (s == 16) { xq = *(const u4*)((const char*)x + out_off(lane)); SB(); }
        if constexpr (nx) an0 = lds_ld<u4>(al + o);
        if constexpr (s >= 22) aw[2 * (s - 22)] = lds_ld<u4>(al + (s + 3 - NWR) * 2048);
        if constexpr (nn >= 1) req(ic<n0>{});
        SB();
        mma32_bf16<s == 0>(acc[2], a1, B0[s]);
        SB();
        if constexpr (nx) an1 = lds_ld<u4>(al + o + 1024);
        if constexpr (s >= 22) aw[2 * (s - 22) + 1] = lds_ld<u4>(al + (s + 3 - NWR) * 2048 + 1024);
        if constexpr (nn >= 2) req(ic<n0 + 1>{});
        SB();
        mma32_bf16<s == 0>(acc[1], a0, B1[s]);
        SB();
        if constexpr (nn == 1) req(ic<n0 + 1>{});
        if constexpr (nn >= 2) req(ic<n0 + 2>{});
        SB();
        mma32_bf16<s == 0>(acc[3], a1, B1[s]);
        SB();
        if constexpr (nn >= 2) req(ic<n0 + 3>{});
        if constexpr (s == 23) { lds_st<float>(sm0 + L_MS + ((w & 1) * 32 + (lane & 31)) * 4, r2); SB(); }
        if constexpr (s == 24) fz = finish_prep(lane, xq);
      });
      static_assert(h_req(25) == 28, "every request is issued by step 24");
      auto put = [&](int k, int e) {
        lds_st<f4>(hr + ((k >> 1) * 4 + e + 8 * (k & 1)) * PSTR + lane * 16, f4{acc[k][4 * e], acc[k][4 * e + 1], acc[k][4 * e + 2], acc[k][4 * e + 3]});
        SB();
      };
      SB();
#pragma unroll
      for (int i = 0; i < 3; i++) { mma32_bf16<false>(acc[0], aw[2 * i], B0[25 + i]); SB(); }
#pragma unroll
      for (int i = 0; i < 3; i++) { mma32_bf16<false>(acc[2], aw[2 * i + 1], B0[25 + i]); SB(); }
      asm volatile("s_nop 8");
      SB();
      put(0, 0); put(0, 1); put(0, 2); put(0, 3);
      mma32_bf16<false>(acc[1], aw[0], B1[25]); SB();
      put(2, 0); put(2, 1);
      mma32_bf16<false>(acc[1], aw[2], B1[26]); SB();
      put(2, 2); put(2, 3);
      mma32_bf16<false>(acc[1], aw[4], B1[27]); SB();
#pragma unroll
      for (int i = 0; i < 3; i++) { mma32_bf16<false>(acc[3], aw[2 * i + 1], B1[25 + i]); SB(); }
      asm volatile("s_nop 8");
      SB();
      put(1, 0); put(1, 1); put(1, 2); put(1, 3);
      put(3, 0); put(3, 1); put(3, 2); put(3, 3);
    }
    WAIT_LGKM0();
    asm volatile("FUSED_TAIL:");
    BAR();
    finish(fz);
  } else {
    LANE_VARS
    const rsrc_t rs = __builtin_amdgcn_make_buffer_rsrc(ws, 0, WS_END, 0x00020000);
    u64 tag;
    float kscale;
    float v_dsk;
    u32 upk[21], vpk[32];
    auto gen_uv = [&](const prm& q) {
      const u32 p_lar = q.lar, p_aim = q.aim, p_ldt = q.ldt, p_cr = q.cr, p_ci = q.ci, p_cr2 = q.cr2, p_ci2 = q.ci2;
      v_dsk = blo(q.dsk);
      const float dt = rbf(__expf(blo(p_ldt)));
      const float lr = -rbf(__expf(blo(p_lar))) * dt * 1.44269504088896f;
      const float fr = blo(p_aim) * dt * 0.15915494309189535f;
      u32 cm = max(max(p_cr & 0x7fffu, p_ci & 0x7fffu), max(p_cr2 & 0x7fffu, p_ci2 & 0x7fffu));
      cm = max(cm, (u32)__builtin_amdgcn_update_dpp(0, (int)cm, 0xB1, 0xf, 0xf, true));
      cm = max(cm, (u32)__builtin_amdgcn_update_dpp(0, (int)cm, 0x4E, 0xf, 0xf, true));
      cm = max(cm, (u32)__builtin_amdgcn_update_dpp(0, (int)cm, 0x141, 0xf, 0xf, true));
      cm = max(cm, (u32)__builtin_amdgcn_update_dpp(0, (int)cm, 0x140, 0xf, 0xf, true));
      cm = xmax32(xmax16(cm)) >> 7;
      const float inv = __uint_as_float((254u - cm) << 23);
      kscale = 2.f * dt * __uint_as_float(cm << 23);
      const cf c1 = cexp_at(lr, fr, 1.f), c64 = cexp_at(lr, fr, 64.f);
      const f2 e1 = f2{c1.r, c1.i};
      f2 v[32], u[21], e2, e4, e256, e512, e1024;
      v[8] = f2{c64.r, c64.i};
      u[0] = f2{blo(p_cr) * inv, blo(p_ci) * inv};
      cmul_pk2(e2, v[16], e1, e1, v[8], v[8]);
      cmul_pk2(u[1], v[24], u[0], e1, v[8], v[16]);
      cmul_pk2(e4, e256, e2, e2, v[16], v[16]);
      cmul_pk2(u[2], u[3], u[0], e2, u[1], e2);
      cmul_pk2(v[1], e512, e4, e4, e256, e256);
      cmul_pk2(u[4], u[5], u[0], e4, u[1], e4);
      cmul_pk2(v[2], e1024, v[1], v[1], e512, e512);
      cmul_pk2(u[6], u[7], u[2], e4, u[3], e4);
      cmul_pk2(v[3], v[4], v[1], v[2], v[2], v[2]);
      cmul_pk2(u[8], u[9], u[0], e256, u[2], e256);
      cmul_pk2(v[5], v[6], v[1], v[4], v[2], v[4]);
      cmul_pk2(u[10], u[11], u[4], e256, u[6], e256);
      cmul_pk2(v[7], u[16], v[3], v[4], u[0], e1024);
      cmul_pk2(u[12], u[13], u[0], e512, u[4], e512);
      cmul_pk2(u[14], u[15], u[8], e512, u[10], e512);
      cmul_pk2(u[17], u[18], u[8], e1024, u[12], e1024);
      cmul_pk2(u[19], u[20], u[14], e1024, u[16], e1024);
#pragma unroll
      for (int j = 1; j < 8; j++) cmul_pk2(v[8 + j], v[16 + j], v[j], v[8], v[j], v[16]);
#pragma unroll
      for (int j = 1; j < 7; j += 2) cmul_pk2(v[24 + j], v[25 + j], v[j], v[24], v[j + 1], v[24]);
      v[31] = cmul_pk(v[7], v[24]);
#pragma unroll
      for (int i = 0; i < 21; i++) upk[i] = pk_f16_conj(u[i]);
      vpk[0] = 0x00003c00u;
#pragma unroll
      for (int j = 1; j < 32; j++) vpk[j] = pk_f16(v[j][0], v[j][1]);
      SB();
    };
    auto put_row = [&](auto J) {
      constexpr int j = decltype(J)::value;
      if constexpr (j == 0) set_m0(sm0 + L_UB - 61440 + w * 4096);
      if constexpr (j == 15) set_m0(ks);
      if constexpr (j < 15) lds_st_tid<61440 + j * 272>(upk[j]);
      else if constexpr (j < 21) lds_st_tid<8704 + (j - 15) * 272>(upk[j]);
      else if constexpr (j < 53) lds_st_tid<(j - 21) * 272>(vpk[j - 21]);
    };
    u4 bf[2][8], tf[2][8];
    u32 ba, pa[8];
    auto ldB = [&](int r, int i) { return lds_ld<u4>(ba + 192 - 64 * r + 512 * i); };
    auto conv_addr = [&]() {
      ba = xs + 2 * (240 + 16 * a16 + 8 * G - 16) - 256 * hw - 192;
      asm("" : "+v"(ba));
#pragma unroll
      for (int k = 0; k < 8; k++) { pa[k] = Pt + 4 * (2031 - a16 + 8 * G) - 512 * hw - 384 - 1024 * k; asm("" : "+v"(pa[k])); }
      SB();
    };
    auto conv_read = [&](int r, int m) {
      if (m >= 2 && m < 10) bf[r & 1][m - 2] = ldB(r, m - 2);
      else {
        const int kk = m < 2 ? 0 : (m - 8) >> 1, e = (m & 1) * 2;
        const u32 z = pa[kk] + 384 - 128 * r + e * 8;
        tf[r & 1][kk][e] = lds_ld<u32>(z); tf[r & 1][kk][e + 1] = lds_ld<u32>(z + 8);
      }
      SB();
    };
    u32 fho = WS_FH + ((ti * 2 + (w >> 1)) * 32 + tj * 2 + (w & 1)) * FS;
    int thr = -1;
    if (!isS) {
      if (lane == 0) {
        lds_st<u32>(sm0 + L_MI + 32 + w * 4, 0u);
        lds_st<u32>(sm0 + L_MI + 48 + w * 4, 0u);
      }
      const int th = w >> 1, fh = w & 1;
      f4 acc[8][4];
      u32 ta[4], xa[2];
#pragma unroll
      for (int cc = 0; cc < 4; cc++) ta[cc] = sm0 + L_WR + (8 * G + qq) * 256 + (((4 * fh + cc) ^ (qq | ((G & 1) << 2))) << 5) + 8 * p;
      const u32 xl = sm0 + L_XR + (th * 64 + a16) * 128;
      xa[0] = xl + ((G ^ (a16 >> 1)) << 4); xa[1] = xl + (((4 + G) ^ (a16 >> 1)) << 4);
      const int un = tj * 2 + fh, ukq = (un * 37) >> 8;
      u32 hs = WS_H + ((ti * 4 + th * 2) * 112 + ukq * 14 + un * 2) * 1024 + lane * 16;
      asm volatile("" : "+v"(ta[0]), "+v"(ta[1]), "+v"(ta[2]), "+v"(ta[3]), "+v"(xa[0]), "+v"(xa[1]), "+v"(hs));
      prm_wait(pg);
      tag = pg.ep + 1;
      asm volatile("" : "+v"(tag));
      gen_uv(pg);
      BAR();
      auto wadr = [&](int i) -> u32 { return ta[seq_n(i) >> 1] + (w_slot(seq_t(i)) - L_WR) + (seq_n(i) & 1) * 8192; };
      auto xadr = [&](int t, int j) -> u32 { return xa[t & 1] + ((t >> 1) % 3) * 16384 + j * 2048; };
      u4 hq[4];
      auto hstore = [&](int c0, int k) { st128_dev(rs, hs + ((k & 1) * 112 + c0 * 7 + (k >> 1)) * 1024, hq[k]); };
      float nm[4];
      f4 cg[2], cu[2];
      auto epi_load = [&](int c0) {
        if (c0 == 0) {
#pragma unroll
          for (int j = 0; j < 4; j++) nm[j] = -lds_ld<float>(sm0 + L_MS + (th * 64 + j * 16 + a16) * 4);
        }
#pragma unroll
        for (int z = 0; z < 2; z++) {
          cg[z] = lds_ld<f4>(sm0 + L_CS + (fh * 64 + (c0 + z) * 16 + 4 * G) * 4);
          cu[z] = lds_ld<f4>(sm0 + L_CS + 512 + (fh * 64 + (c0 + z) * 16 + 4 * G) * 4);
        }
        SB();
      };
      auto epilogue = [&](int c0) {
#pragma unroll
        for (int cc = c0; cc < c0 + 2; cc++) {
#pragma unroll
          for (int pp = 0; pp < 2; pp++) {
            u2 hv[2];
#pragma unroll
            for (int z = 0; z < 2; z++) {
              const int j = 2 * pp + z;
              const f4 pr = (acc[2 * cc][j] + cg[cc - c0] * nm[j]) * (acc[2 * cc + 1][j] + cu[cc - c0] * nm[j]);
              hv[z] = u2{pk_bf16(pr[0], pr[1]), pk_bf16(pr[2], pr[3])};
              if (c0 == 2) {
                SB();
#pragma unroll
                for (int m = 0; m < 3; m++) conv_read(0, 3 * (4 * (cc - c0) + 2 * pp + z) + m);
              }
            }
            auto r0 = __builtin_amdgcn_permlane16_swap(hv[0][0], hv[1][0], false, false);
            auto r1 = __builtin_amdgcn_permlane16_swap(hv[0][1], hv[1][1], false, false);
            hq[2 * (cc - c0) + pp] = u4{r0[0], r1[0], r0[1], r1[1]};
            if (c0 == 2 || (cc == c0 && pp == 0)) hstore(c0, 2 * (cc - c0) + pp);
          }
        }
      };
      u4 wq[4], xf[4], yf[4];
      wq[0] = cat(lds_tr(wadr(0)), lds_tr(wadr(0) + 1024));
      SB();
#pragma unroll
      for (int j = 0; j < 4; j++) xf[j] = lds_ld<u4>(xadr(0, j));
      SB();
#pragma unroll
      for (int i = 1; i < 3; i++) wq[i] = cat(lds_tr(wadr(i)), lds_tr(wadr(i) + 1024));
      SB();
      static_for<0, 128>([&](auto I) {
        constexpr int i = decltype(I)::value, t = seq_t(i), n = seq_n(i);
        constexpr bool full = i < 96;
        constexpr int f1 = full ? (n < 5 ? i + 3 : n == 6 ? i + 2 : n == 7 ? i + 3 : -1) : (i + 3 < 128 ? i + 3 : -1);
        constexpr int f2i = full && n == 6 ? i + 3 : -1;
        constexpr int pos = full ? n : (n & 3);
        constexpr int tn = full ? t + 1 : (i < 124 ? seq_t(i - pos + 4) : -1);
        if constexpr (i == 100 || i == 104) { BAR(); }
        const u4 a = wq[i & 3];
        constexpr int q = i - 25, r0 = 6 * q, ra = 1, rb = 2, rc = 1, rd = 2;
        auto rows = [&](auto A, auto Nn) { if constexpr (q >= 0 && q < 9) static_for<decltype(A)::value, decltype(A)::value + decltype(Nn)::value>(put_row); };
        u2 lo, hi, lo2, hi2;
        mma_bf16<(i < 8)>(acc[n][0], a, xf[0]);
        SB();
        if constexpr (f1 >= 0) lo = lds_tr(wadr(f1));
        rows(ic<r0>{}, ic<ra>{});
        SB();
        mma_bf16<(i < 8)>(acc[n][1], a, xf[1]);
        SB();
        if constexpr (f1 >= 0) hi = lds_tr(wadr(f1) + 1024);
        rows(ic<r0 + ra>{}, ic<rb>{});
        SB();
        mma_bf16<(i < 8)>(acc[n][2], a, xf[2]);
        SB();
        if constexpr (full && pos < 4) yf[pos] = lds_ld<u4>(xadr(tn, pos));
        if constexpr (!full && pos < 2 && tn >= 0) yf[2 * pos] = lds_ld<u4>(xadr(tn, 2 * pos));
        if constexpr (f2i >= 0) lo2 = lds_tr(wadr(f2i));
        rows(ic<r0 + ra + rb>{}, ic<rc>{});
        SB();
        mma_bf16<(i < 8)>(acc[n][3], a, xf[3]);
        SB();
        if constexpr (!full && pos < 2 && tn >= 0) yf[2 * pos + 1] = lds_ld<u4>(xadr(tn, 2 * pos + 1));
        if constexpr (f2i >= 0) hi2 = lds_tr(wadr(f2i) + 1024);
        rows(ic<r0 + ra + rb + rc>{}, ic<rd>{});
        if constexpr (f1 >= 0) wq[f1 & 3] = cat(lo, hi);
        if constexpr (f2i >= 0) wq[f2i & 3] = cat(lo2, hi2);
        SB();
        if constexpr (full && n == 5) {
          WAIT_LGKM0();
          BAR();
        }
        if constexpr ((full && n == 7) || (!full && pos == 3 && tn >= 0)) {
#pragma unroll
          for (int j = 0; j < 4; j++) xf[j] = yf[j];
        }
        if constexpr (i == 110) { BAR(); epi_load(0); }
        if constexpr (i == 111) { mfma_done(); epilogue(0); SB(); }
        if constexpr (i >= 112 && i < 115) { hstore(0, i - 111); SB(); }
        if constexpr (i == 125) epi_load(2);
        if constexpr (i == 126) conv_addr();
      });
      BAR();
      thr = 4;
      if (vm_pending() == 0) {
        wait_vm<0>();
        if (lane == 0) flag_store(ws, fho, tag);
        fho += 1024 * FS;
        thr = 0;
      }
      mfma_done();
      epilogue(2);
    } else {
      const u32 sreg = sm0 + L_WR + w * 16640;
      WAIT_LGKM0();
      BAR();
      prm ps;
      prm_load(ps, (ch * NM + hw * 64 + lane) * 2, ch * 2, lar, aim, cr, ci, ldt, dsk, ws);
      prm_wait(ps);
      tag = ps.ep + 1;
      u4 xr[16];
      const u32 xa0 = sreg + a16 * 128 + ((G ^ (a16 >> 1)) << 4), xa1 = sreg + a16 * 128 + (((4 + G) ^ (a16 >> 1)) << 4);
#pragma unroll
      for (int k = 0; k < 16; k++) xr[k] = lds_ld<u4>(((k & 1) ? xa1 : xa0) + (k >> 1) * 2048);
      float s1 = 0.f, s2 = 0.f;
      const bf2 ones = as_bf2(0x3f803f80u);
#pragma unroll
      for (int k = 0; k < 16; k++)
#pragma unroll
        for (int e = 0; e < 4; e++) {
          bf2 v = as_bf2(xr[k][e]);
          s1 = __builtin_amdgcn_fdot2_f32_bf16(v, ones, s1, false);
          s2 = __builtin_amdgcn_fdot2_f32_bf16(v, v, s2, false);
        }
      const float mean = xsum32(xsum16(s1)) * (1.f / D);
      const float var = fmaxf(xsum32(xsum16(s2)) * (1.f / D) - mean * mean, 0.f);
      const float sd = rbf(__builtin_amdgcn_sqrtf(rbf(rbf(var) + 1e-5f)));
      const float rsd = __builtin_amdgcn_rcpf(sd), mb = rbf(mean), rc = -mb * rsd;
      if (lane < 16) st64_dev(rs, WS_SS + (tb * 64 + w * 16 + lane) * 8, u2{__float_as_uint(mb), __float_as_uint(rsd * rsd)});
      WAIT_LGKM0();
      const u32 ra = sreg + (8 * (G & 1) + qq) * 1040 + 32 * (G >> 1) + 8 * p;
      const u32 xo = WS_XT + ((4 * (G >> 1) + (a16 >> 2)) * 128 + tb * 4 + w) * 128 + (a16 & 3) * 32 + 16 * (G & 1);
#pragma unroll
      for (int k = 0; k < 17; k++) {
        if (k < 16) {
          u4 o;
#pragma unroll
          for (int e = 0; e < 4; e++) o[e] = pk_f16(__builtin_fmaf(blo(xr[k][e]), rsd, rc), __builtin_fmaf(bhi(xr[k][e]), rsd, rc));
          lds_st<u4>(sreg + a16 * 1040 + k * 64 + G * 16, o);
          xr[k] = cat(lds_tr(ra + k * 64), lds_tr(ra + k * 64 + 4160));
        }
        if (k > 0) st128_dev(rs, xo + (k - 1) * (8 * 128 * 128), xr[k - 1]);
        if (k == 6) {
          wait_vm<6>();
          if (lds_arrive(sm0 + L_MI + 56, 4, lane) && lane == 0) flag_store(ws, WS_FS + tb * FS, tag);
        }
        SB();
      }
      gen_uv(ps);
      wait_vm<0>();
      if (lds_arrive(sm0 + L_MI + 48, 4, lane) && lane == 0) flag_store(ws, WS_FX + tb * FS, tag);
      BAR();
      static_for<0, 53>(put_row);
      WAIT_LGKM0();
      BAR();
      if (w == 0 && lane == 0) flag_store(ws, WS_FA + tb * FS, tag);
      BAR();
      if (tb == 0 && w == 0) {
#pragma unroll 1
        for (int k = 0; k < 4; k++) {
          const int i = k * 64 + lane;
          flag_wait<8>(ws, i < 224 ? WS_FC + i * FS : WS_FA + (i - 224) * FS, tag);
        }
        if (lane == 0) flag_store(ws, WS_EP, tag);
      }
      BAR();
      conv_addr();
#pragma unroll
      for (int m = 0; m < 24; m++) conv_read(0, m);
    }
    u4 xq;
    f4 cac[8];
    float sk[16];
    u32 mine = scr(w) + lane * 16, oth = scr(w ^ 1), pfa = sm0 + L_MI + 32 + w * 4;
    asm volatile("" : "+v"(mine), "+v"(oth), "+v"(pfa));
    {
      auto publish_now = [&]() {
        wait_vm<4>();
        if (thr == 1) wait_vm<1>();
        if (thr == 0) wait_vm<0>();
        if (lane == 0) flag_store(ws, fho, tag);
        fho += 1024 * FS;
        thr = thr == 4 ? 1 : -1;
      };
      u2 xn2[4];
      if (thr == 4) publish_now();
      SB();
      static_for<0, 144>([&](auto I) {
        constexpr int i = decltype(I)::value, r = i / 36, n = i % 36;
        constexpr int k = n < 8 ? 0 : n < 15 ? 1 : n < 21 ? 2 : n < 26 ? 3 : n < 30 ? 4 : n < 33 ? 5 : n < 35 ? 6 : 7;
        constexpr int j = k + n - (k * (17 - k)) / 2;
        mma_f16<(i < 8)>(cac[j], tf[r & 1][k], bf[r & 1][j - k]);
        SB();
        if constexpr (r < 3 && n % 3 != 2) conv_read(r + 1, n - n / 3);
        if constexpr (i == PUBN) { if (thr >= 0) publish_now(); xq = *(const u4*)((const char*)x + out_off(lane)); SB(); }
        if constexpr (i >= 108 && i < 112) { xn2[i - 108] = lds_ld<u2>(xs + 480 + 2 * (1024 * hw + 256 * (i - 108) + 16 * a16 + 4 * G)); SB(); }
        if constexpr (i >= 120 && i < 136) {
          constexpr int q = i - 120;
          const h2 hx = as_h2(xn2[q >> 2][(q >> 1) & 1]);
          sk[q] = mul1(v_dsk, (float)hx[q & 1]);
          SB();
        }
      });
      mfma_done();
    }
    __builtin_amdgcn_s_setprio(1);
    {
      f4 keep[4];
      if (hw) {
#pragma unroll
        for (int j = 0; j < 4; j++) { lds_st<f4>(mine + j * 1024, cac[j]); keep[j] = cac[4 + j]; }
        asm volatile("" ::: "memory");
      } else {
#pragma unroll
        for (int j = 0; j < 4; j++) { lds_st<f4>(mine + j * 1024, cac[4 + j]); keep[j] = cac[j]; }
      }
      WAIT_LGKM0();
      *(volatile LAS u32*)(size_t)pfa = 1u;
      SB();
#pragma unroll
      for (int j = 0; j < 4; j++)
#pragma unroll
        for (int e = 0; e < 4; e++) sk[4 * j + e] = fma1(keep[j][e], kscale, sk[4 * j + e]);
      SB();
#pragma unroll 1
      for (int s = 0; s < SPIN_CAP; s++) {
        if (*(volatile LAS u32*)(size_t)(pfa ^ 4) != 0u) break;
      }
      asm volatile("" ::: "memory");
      u2 o[4];
#pragma unroll
      for (int j = 0; j < 4; j++) {
        const f4 v = lds_ld<f4>(oth + j * 1024 + lane * 16);
        float r[4];
#pragma unroll
        for (int e = 0; e < 4; e++) r[e] = fma1(v[e], kscale, sk[4 * j + e]);
        o[j] = u2{pk_bf16(r[0], r[1]), pk_bf16(r[2], r[3])};
      }
      const u64 ym = (tag & 0xffull) << (8 * ((a16 & 3) * 2 + (G >> 1)));
      const u32 yo = WS_YT + ((16 * hw + 4 * (G & 1) + (a16 >> 2)) * 512 + ch) * 128 + (a16 & 3) * 32 + (G >> 1) * 16;
      u2 ck[2];
#pragma unroll
      for (int j = 0; j < 4; j += 2) {
        auto r0 = __builtin_amdgcn_permlane16_swap(o[j][0], o[j + 1][0], false, false);
        auto r1 = __builtin_amdgcn_permlane16_swap(o[j][1], o[j + 1][1], false, false);
        st128_dev(rs, yo + j * (4 * 512 * 128), u4{r0[0] ^ (u32)ym, r1[0] ^ (u32)(ym >> 32), r0[1], r1[1]});
        ck[j >> 1] = u2{xxor32(xor_dpp<0x4E>(xor_dpp<0xB1>(r0[0] ^ r0[1]))), xxor32(xor_dpp<0x4E>(xor_dpp<0xB1>(r1[0] ^ r1[1])))};
      }
      const u2 c = (G >> 1) ? ck[1] : ck[0];
      if ((a16 & 3) == 0) st64_dev(rs, WS_YC + (ch * 32 + 16 * hw + 4 * G + (a16 >> 2)) * 8, u2{c[0] ^ (u32)tag, c[1] ^ (u32)(tag >> 32)});
    }
    {
      const int c0 = cb * 64 + 16 * w;
      const u32 ys = WS_YT + (tb * 512 + c0) * 128, cs_ = WS_YC + (c0 * 32 + tb) * 8;
      u4 yg[2];
      u2 yc[2];
      bool pend[2] = {true, true};
      const u64 ym = (tag & 0xffull) << (8 * (lane & 7));
      const u32 m0 = (u32)ym, m1 = (u32)(ym >> 32);
      __builtin_amdgcn_s_sleep(YSLEEP);
      if (isS) __builtin_amdgcn_s_sleep(SSLEEP);
#pragma unroll 1
      for (int s = 0; s < SPIN_CAP; s++) {
#pragma unroll
        for (int i = 0; i < 2; i++)
          if (pend[i]) { yg[i] = ld128_dev(rs, lane * 16, ys + i * 1024); yc[i] = ld64_dev(rs, (lane >> 3) * 256, cs_ + i * 2048); }
        asm volatile("" ::: "memory");
#pragma unroll
        for (int i = 0; i < 2; i++) {
          const u32 a = xor_dpp<0x141>(xor_dpp<0x4E>(xor_dpp<0xB1>(yg[i][0] ^ m0 ^ yg[i][2])));
          const u32 c = xor_dpp<0x141>(xor_dpp<0x4E>(xor_dpp<0xB1>(yg[i][1] ^ m1 ^ yg[i][3])));
          pend[i] = ((a ^ yc[i][0] ^ (u32)tag) | (c ^ yc[i][1] ^ (u32)(tag >> 32))) != 0u;
        }
        if (!__any(pend[0] || pend[1])) break;
        __builtin_amdgcn_s_sleep(4);
      }
      lds_st<u4>(oth + lane * 16, u4{yg[0][0] ^ m0, yg[0][1] ^ m1, yg[0][2], yg[0][3]});
      lds_st<u4>(oth + 1024 + lane * 16, u4{yg[1][0] ^ m0, yg[1][1] ^ m1, yg[1][2], yg[1][3]});
      const u32 ya = oth + qq * 128 + G * 32 + 8 * p;
      const u4 t0 = cat(lds_tr(ya), lds_tr(ya + 512)), t1 = cat(lds_tr(ya + 1024), lds_tr(ya + 1536));
      lds_st<u4>(oth + 2048 + lane * 32, t0);
      lds_st<u4>(oth + 2048 + lane * 32 + 16, t1);
    }
    const fin fz = finish_prep(lane, xq);
    WAIT_LGKM0();
    BAR();
    finish(fz);
  }
}

extern "C" void kernel_launch(void* const* d_in, const int* in_sizes, int n_in, void* d_out, int out_size,
                              void* d_ws, size_t ws_size, hipStream_t stream) {
  (void)hipFuncSetAttribute((const void*)fused, hipFuncAttributeMaxDynamicSharedMemorySize, LDS_BYTES);
  fused<<<256, 512, LDS_BYTES, stream>>>((const u16*)d_in[0], (const u16*)d_in[1], (const u16*)d_in[2], (const u16*)d_in[3],
                                         (const u16*)d_in[4], (const u16*)d_in[5], (const u16*)d_in[6], (const u16*)d_in[8],
                                         (const u16*)d_in[9], (const u16*)d_in[10], (char*)d_ws, (u16*)d_out);
}
```
